```python
import torch

def _softmax(x, axis=-1):
    return torch.softmax(x, dim=axis)
def _gelu(x):
    return torch.nn.functional.gelu(x, approximate="tanh")
def _silu(x):
    return torch.nn.functional.silu(x)
def _sigmoid(x):
    return torch.sigmoid(x)
def _layernorm(x, axis=-1, eps=1e-5):
    m = x.mean(dim=axis, keepdim=True)
    v = x.var(dim=axis, keepdim=True, unbiased=False)
    return (x - m) / torch.sqrt(v + eps)
def _scatter_add(tgt, idx, src, axis=0):
    return tgt.clone().index_add_(axis, idx, src)
def _segment_sum(src, seg, n):
    out = torch.zeros((n,) + src.shape[1:], dtype=src.dtype)
    return out.index_add_(0, seg, src)
def _pad(x, pw):
    flat = tuple(v for pair in reversed(pw) for v in pair)
    return torch.nn.functional.pad(x, flat)

kernel_name = 'sequential_preLN__compressed_kv_cla__swiglu_ffn__decode'

def setup_inputs():

    _w = (lambda a: (lambda t: t.to(torch.bfloat16) if t.is_floating_point() else t)(torch.as_tensor(a)))
    def _A_setup():
        B, H, Sq, Sk, D, M = 4, 32, 1, 4096, 128, 512
        Dc = 4 * D
        return {
            "x": _w((lambda *s: torch.randn(*s, dtype=torch.bfloat16) * 0.05)(B, Sq, M)),
            "c_kv": _w((lambda *s: torch.randn(*s, dtype=torch.bfloat16) * 0.4)(B, Sk, Dc)),
            "w_q": _w((lambda *s: torch.randn(*s, dtype=torch.bfloat16) * 3.2)(M, H, D) * (M ** -0.5)),
            "w_uk": _w((lambda *s: torch.randn(*s, dtype=torch.bfloat16) * 3.2)(Dc, H, D) * (Dc ** -0.5)),
            "w_uv": _w((lambda *s: torch.randn(*s, dtype=torch.bfloat16) * 0.4)(Dc, H, D) * (Dc ** -0.5)),
            "w_o": _w((lambda *s: torch.randn(*s, dtype=torch.bfloat16) * 0.4)(H, D, M) * ((H * D) ** -0.5)),
        }
    _A_in = _A_setup()

    def _B_setup():
        B, S, D, F = 4, 1, 512, 1792
        return {
            "x": _w((lambda *s: torch.randn(*s, dtype=torch.bfloat16) * 0.05)(B, S, D)),
            "w_gate": _w((lambda *s: torch.randn(*s, dtype=torch.bfloat16) * 0.05)(D, F)),
            "w_up": _w((lambda *s: torch.randn(*s, dtype=torch.bfloat16) * 0.05)(D, F)),
            "w_down": _w((lambda *s: torch.randn(*s, dtype=torch.bfloat16) * 0.05)(F, D)),
        }
    _B_in = _B_setup()

    return {
        "A_x": _A_in["x"],
        "A_c_kv": _A_in["c_kv"],
        "A_w_q": _A_in["w_q"],
        "A_w_uk": _A_in["w_uk"],
        "A_w_uv": _A_in["w_uv"],
        "A_w_o": _A_in["w_o"],
        "B_x": _B_in["x"],
        "B_w_gate": _B_in["w_gate"],
        "B_w_up": _B_in["w_up"],
        "B_w_down": _B_in["w_down"],
    }

def reference(A_x, A_c_kv, A_w_q, A_w_uk, A_w_uv, A_w_o, B_x, B_w_gate, B_w_up, B_w_down):
    def _A_ref(x, c_kv, w_q, w_uk, w_uv, w_o):
        B, Sq, M = x.shape[0], x.shape[1], x.shape[2]
        Sk = c_kv.shape[1]
        H, D = w_q.shape[1], w_q.shape[2]
        q = torch.einsum("bqm,mhd->bhqd", x, w_q)
        k = torch.einsum("bkc,chd->bhkd", c_kv, w_uk)
        v = torch.einsum("bkc,chd->bhkd", c_kv, w_uv)
        scores = torch.einsum("bhqd,bhkd->bhqk", q, k) * (D ** -0.5)
        off = Sk - Sq
        qi = torch.arange(Sq)[:, None]
        kj = torch.arange(Sk)[None, :]
        cmask = kj <= (qi + off)
        scores = torch.where(cmask[None, None], scores, -1e9)
        p = _softmax(scores, axis=-1)
        ctx = torch.einsum("bhqk,bhkd->bhqd", p, v)
        return torch.einsum("bhqd,hdm->bqm", ctx, w_o)

    def _B_ref(x, w_gate, w_up, w_down):
        g = _silu(x @ w_gate)
        u = x @ w_up
        return (g * u) @ w_down

    x = A_x
    h = x + _A_ref(_layernorm(x, axis=-1), A_c_kv, A_w_q, A_w_uk, A_w_uv, A_w_o)
    h2 = h + _B_ref(_layernorm(h, axis=-1), B_w_gate, B_w_up, B_w_down)
    return h2
```

```cpp
#include <hip/hip_runtime.h>
#include <hip/hip_bf16.h>
#include <stdint.h>
typedef unsigned short u16;
__device__ __forceinline__ float bf2f(u16 v){ return __uint_as_float(((unsigned)v)<<16); }
__device__ __forceinline__ u16 f2bf(float f){ unsigned u=__float_as_uint(f); u += 0x7FFFu + ((u>>16)&1u); return (u16)(u>>16); }
constexpr int B=4,H=32,SK=4096,D=128,M=512,DC=512,F=1792;

__device__ float block_sum(float v, float* red){
  for(int o=32;o>=1;o>>=1) v += __shfl_xor(v,o);
  int w=threadIdx.x>>6; __syncthreads(); if((threadIdx.x&63)==0) red[w]=v; __syncthreads();
  float s=0; for(int i=0;i<(int)(blockDim.x>>6);i++) s+=red[i]; return s;
}
__global__ void k_q(const u16* x,const u16* wq,float* q){
  __shared__ float xn[B][M]; __shared__ float red[8];
  for(int b=0;b<B;b++){
    float v0=bf2f(x[b*M+threadIdx.x]), v1=bf2f(x[b*M+256+threadIdx.x]);
    float mean=block_sum(v0+v1,red)/M; float d0=v0-mean,d1=v1-mean;
    float var=block_sum(d0*d0+d1*d1,red)/M; float r=rsqrtf(var+1e-5f);
    xn[b][threadIdx.x]=d0*r; xn[b][256+threadIdx.x]=d1*r;
  }
  __syncthreads();
  int n=blockIdx.x*256+threadIdx.x; float a[B]={0,0,0,0};
  for(int m=0;m<M;m++){ float w=bf2f(wq[(size_t)m*4096+n]); for(int b=0;b<B;b++) a[b]+=xn[b][m]*w; }
  for(int b=0;b<B;b++) q[b*4096+n]=a[b];
}
__global__ void k_qa(const float* q,const u16* wuk,float* qa){
  int h=blockIdx.x, c=blockIdx.y*256+threadIdx.x; float a[B]={0,0,0,0};
  for(int d=0;d<D;d++){ float w=bf2f(wuk[((size_t)c*H+h)*D+d]); for(int b=0;b<B;b++) a[b]+=q[b*4096+h*D+d]*w; }
  for(int b=0;b<B;b++) qa[(b*H+h)*DC+c]=a[b]*0.08838834764831845f;
}
__global__ void k_s(const float* qa,const u16* ckv,float* s){
  int b=blockIdx.y, k=blockIdx.x*64+(threadIdx.x&63), hg=threadIdx.x>>6;
  float a[8]; for(int i=0;i<8;i++) a[i]=0;
  for(int c=0;c<DC;c++){ float v=bf2f(ckv[((size_t)b*SK+k)*DC+c]); for(int i=0;i<8;i++) a[i]+=qa[(b*H+hg*8+i)*DC+c]*v; }
  for(int i=0;i<8;i++) s[((size_t)b*H+hg*8+i)*SK+k]=a[i];
}
__global__ void k_sm(float* s){
  __shared__ float red[8]; float* r=s+(size_t)blockIdx.x*SK; float m=-1e30f;
  for(int i=threadIdx.x;i<SK;i+=256) m=fmaxf(m,r[i]);
  for(int o=32;o>=1;o>>=1) m=fmaxf(m,__shfl_xor(m,o));
  if((threadIdx.x&63)==0) red[threadIdx.x>>6]=m; __syncthreads();
  m=fmaxf(fmaxf(red[0],red[1]),fmaxf(red[2],red[3])); __syncthreads();
  float l=0; for(int i=threadIdx.x;i<SK;i+=256){ float e=__expf(r[i]-m); r[i]=e; l+=e; }
  l=block_sum(l,red); float inv=1.f/l;
  for(int i=threadIdx.x;i<SK;i+=256) r[i]*=inv;
}
__global__ void k_o(const float* p,const u16* ckv,float* o){
  int bh=blockIdx.x, b=bh/H, c=blockIdx.y*256+threadIdx.x; float a=0;
  for(int k=0;k<SK;k++) a+=p[(size_t)bh*SK+k]*bf2f(ckv[((size_t)b*SK+k)*DC+c]);
  o[bh*DC+c]=a;
}
__global__ void k_ctx(const float* o,const u16* wuv,float* ctx){
  int h=blockIdx.x, d=threadIdx.x; float a[B]={0,0,0,0};
  for(int c=0;c<DC;c++){ float w=bf2f(wuv[((size_t)c*H+h)*D+d]); for(int b=0;b<B;b++) a[b]+=o[(b*H+h)*DC+c]*w; }
  for(int b=0;b<B;b++) ctx[b*4096+h*D+d]=a[b];
}
__global__ void k_h(const float* ctx,const u16* wo,const u16* x,float* h1){
  int m=blockIdx.x*64+threadIdx.x; int b=blockIdx.y; float a=0;
  for(int k=0;k<4096;k++) a+=ctx[b*4096+k]*bf2f(wo[(size_t)k*M+m]);
  h1[b*M+m]=a+bf2f(x[b*M+m]);
}
__global__ void k_gu(const float* h1,const u16* wg,const u16* wu,float* act){
  __shared__ float xn[B][M]; __shared__ float red[8];
  for(int b=0;b<B;b++){
    float v0=h1[b*M+threadIdx.x], v1=h1[b*M+256+threadIdx.x];
    float mean=block_sum(v0+v1,red)/M; float d0=v0-mean,d1=v1-mean;
    float var=block_sum(d0*d0+d1*d1,red)/M; float r=rsqrtf(var+1e-5f);
    xn[b][threadIdx.x]=d0*r; xn[b][256+threadIdx.x]=d1*r;
  }
  __syncthreads();
  int f=blockIdx.x*256+threadIdx.x; float g[B]={0,0,0,0},u[B]={0,0,0,0};
  for(int m=0;m<M;m++){ float a=bf2f(wg[(size_t)m*F+f]), c=bf2f(wu[(size_t)m*F+f]); for(int b=0;b<B;b++){ g[b]+=xn[b][m]*a; u[b]+=xn[b][m]*c; } }
  for(int b=0;b<B;b++) act[b*F+f]=g[b]/(1.f+__expf(-g[b]))*u[b];
}
__global__ void k_dn(const float* act,const u16* wd,const float* h1,u16* out){
  int m=blockIdx.x*64+threadIdx.x; int b=blockIdx.y; float a=0;
  for(int k=0;k<F;k++) a+=act[b*F+k]*bf2f(wd[(size_t)k*M+m]);
  out[b*M+m]=f2bf(a+h1[b*M+m]);
}
extern "C" void kernel_launch(void* const* d_in, const int* in_sizes, int n_in, void* d_out, int out_size, void* d_ws, size_t ws_size, hipStream_t stream){
  const u16* x=(const u16*)d_in[0]; const u16* ckv=(const u16*)d_in[1]; const u16* wq=(const u16*)d_in[2]; const u16* wuk=(const u16*)d_in[3];
  const u16* wuv=(const u16*)d_in[4]; const u16* wo=(const u16*)d_in[5]; const u16* wg=(const u16*)d_in[7]; const u16* wu=(const u16*)d_in[8]; const u16* wd=(const u16*)d_in[9];
  float* ws=(float*)d_ws; float* q=ws; float* qa=q+B*4096; float* s=qa+B*H*DC; float* o=s+(size_t)B*H*SK; float* ctx=o+B*H*DC; float* h1=ctx+B*4096; float* act=h1+B*M;
  k_q<<<16,256,0,stream>>>(x,wq,q);
  k_qa<<<dim3(H,2),256,0,stream>>>(q,wuk,qa);
  k_s<<<dim3(SK/64,B),256,0,stream>>>(qa,ckv,s);
  k_sm<<<B*H,256,0,stream>>>(s);
  k_o<<<dim3(B*H,2),256,0,stream>>>(s,ckv,o);
  k_ctx<<<H,128,0,stream>>>(o,wuv,ctx);
  k_h<<<dim3(8,B),64,0,stream>>>(ctx,wo,x,h1);
  k_gu<<<7,256,0,stream>>>(h1,wg,wu,act);
  k_dn<<<dim3(8,B),64,0,stream>>>(act,wd,h1,(u16*)d_out);
}
```

```cpp
#include <hip/hip_runtime.h>
#include <stdint.h>

typedef unsigned u32;
typedef unsigned short u16;
typedef u32 v4u __attribute__((ext_vector_type(4)));
typedef u32 v2u __attribute__((ext_vector_type(2)));
typedef float f4 __attribute__((ext_vector_type(4)));
typedef float f2 __attribute__((ext_vector_type(2)));
typedef short s8 __attribute__((ext_vector_type(8)));
typedef short s4 __attribute__((ext_vector_type(4)));
typedef __bf16 bf2 __attribute__((ext_vector_type(2)));
#define LAS __attribute__((address_space(3)))
#define DI __device__ __forceinline__

constexpr u32 WS_EPOCH = 0;
constexpr u32 WS_Q = 4096;
constexpr u32 WS_QA = WS_Q + 262144;
constexpr u32 WS_ST = WS_QA + 163840;
constexpr u32 WS_AP = WS_ST + 65536;
constexpr u32 WS_H1 = WS_AP + 1048576;
constexpr u32 WS_FP = WS_H1 + 8192;
constexpr u32 WS_PO = WS_FP + 112 * 8192;
constexpr u32 WS_END = WS_PO + 16 * 4 * 128 * 640;
constexpr u32 WS_STAMP = WS_END;
constexpr int SPIN_CAP = 1 << 17;
constexpr u32 LDS_BYTES = 163840;
#define CODE_PF_BYTES 22400
#define CODE_PF_LINES ((CODE_PF_BYTES + 8191) / 8192)

#define PF_POL "nt"
#define NOPF 1
#define NOTOUCH 1
#define PODELAY 0
DI v4u mk_srd(const void* p, u32 bytes) {
  unsigned long long a = (unsigned long long)p;
  return v4u{(u32)a, (u32)(a >> 32) & 0xFFFFu, bytes, 0x00020000u};
}
DI void dma16(v4u srd, u32 voff, u32 soff, u32 lds) {
  u32 keep;
  asm volatile("s_mov_b32 %0, m0\n\ts_mov_b32 m0, %4\n\ts_nop 2\n\tbuffer_load_dwordx4 %1, %2, %3 offen nt lds\n\ts_mov_b32 m0, %0"
               : "=&s"(keep) : "v"(voff), "s"(srd), "s"(soff), "s"(lds) : "memory");
}
DI void dma4(v4u srd, u32 voff, u32 soff, u32 lds) {
  u32 keep;
  asm volatile("s_mov_b32 %0, m0\n\ts_mov_b32 m0, %4\n\ts_nop 2\n\tbuffer_load_dword %1, %2, %3 offen " PF_POL " lds\n\ts_mov_b32 m0, %0"
               : "=&s"(keep) : "v"(voff), "s"(srd), "s"(soff), "s"(lds) : "memory");
}
DI void dma4c(v4u srd, u32 voff, u32 soff, u32 lds) {
  u32 keep;
  asm volatile("s_mov_b32 %0, m0\n\ts_mov_b32 m0, %4\n\ts_nop 2\n\tbuffer_load_dword %1, %2, %3 offen sc1 lds\n\ts_mov_b32 m0, %0"
               : "=&s"(keep) : "v"(voff), "s"(srd), "s"(soff), "s"(lds) : "memory");
}
#define VMCNT(n) asm volatile("s_waitcnt vmcnt(" #n ")" ::: "memory")
#define BAR() asm volatile("s_waitcnt lgkmcnt(0)\n\ts_barrier" ::: "memory")
#define CFENCE() asm volatile("" ::: "memory")
#define SCHED() __builtin_amdgcn_sched_barrier(0)

DI v4u lds128(u32 a) { return *(LAS v4u*)(uintptr_t)a; }
DI v2u lds64(u32 a) { return *(LAS v2u*)(uintptr_t)a; }
DI float lds32f(u32 a) { return *(LAS float*)(uintptr_t)a; }
DI u32 lds16(u32 a) { return *(LAS u16*)(uintptr_t)a; }
DI void st128(u32 a, v4u v) { *(LAS v4u*)(uintptr_t)a = v; }
DI void st64(u32 a, v2u v) { *(LAS v2u*)(uintptr_t)a = v; }
DI void st32f(u32 a, float v) { *(LAS float*)(uintptr_t)a = v; }
DI void st16(u32 a, u16 v) { *(LAS u16*)(uintptr_t)a = v; }
DI v2u tr64(u32 a) { return __builtin_bit_cast(v2u, __builtin_amdgcn_ds_read_tr16_b64_v4i16((LAS s4*)(uintptr_t)a)); }
DI v4u trfrag(u32 a0, u32 a1) { v2u x = tr64(a0), y = tr64(a1); return v4u{x.x, x.y, y.x, y.y}; }

DI float asf(u32 v) { return __builtin_bit_cast(float, v); }
DI u32 asu(float v) { return __builtin_bit_cast(u32, v); }
DI float bflo(u32 v) { return asf(v << 16); }
DI float bfhi(u32 v) { return asf(v & 0xFFFF0000u); }
DI u32 pkbf(float a, float b) { f2 v = {a, b}; return __builtin_bit_cast(u32, __builtin_convertvector(v, bf2)); }
DI f4 mfma32(v4u a, v4u b, f4 c) { return __builtin_amdgcn_mfma_f32_16x16x32_bf16(__builtin_bit_cast(s8, a), __builtin_bit_cast(s8, b), c, 0, 0, 0); }
DI f4 mfma16(v2u a, v2u b, f4 c) { return __builtin_amdgcn_mfma_f32_16x16x16bf16_1k(__builtin_bit_cast(s4, a), __builtin_bit_cast(s4, b), c, 0, 0, 0); }
template <int N> DI float ror(float v) { return asf((u32)__builtin_amdgcn_update_dpp(0, (int)asu(v), 0x120 + N, 0xf, 0xf, false)); }
DI float sum16x(float v) { auto r = __builtin_amdgcn_permlane16_swap(asu(v), asu(v), false, false); return asf(r[0]) + asf(r[1]); }
DI float sum32x(float v) { auto r = __builtin_amdgcn_permlane32_swap(asu(v), asu(v), false, false); return asf(r[0]) + asf(r[1]); }
DI float mx(float a, float b) { return __builtin_elementwise_maximum(a, b); }
DI float mx4(f4 a) { return mx(mx(a[0], a[1]), mx(a[2], a[3])); }
template <int C> DI float qp(float v) { return asf((u32)__builtin_amdgcn_update_dpp(0, (int)asu(v), C, 0xf, 0xf, false)); }
DI float max16x(float v) { auto r = __builtin_amdgcn_permlane16_swap(asu(v), asu(v), false, false); return mx(asf(r[0]), asf(r[1])); }
DI float max32x(float v) { auto r = __builtin_amdgcn_permlane32_swap(asu(v), asu(v), false, false); return mx(asf(r[0]), asf(r[1])); }
DI float rowsum16(float v) { v += ror<8>(v); v += ror<4>(v); v += ror<2>(v); v += ror<1>(v); return v; }
DI float rowmax16(float v) { v = mx(v, ror<8>(v)); v = mx(v, ror<4>(v)); v = mx(v, ror<2>(v)); v = mx(v, ror<1>(v)); return v; }
DI u32 fsw(u32 row) { return ((row & 3u) | (((row >> 3) & 1u) << 2)) << 1; }
DI f4 sel4(bool c, f4 a, f4 b) { return f4{c ? a[0] : b[0], c ? a[1] : b[1], c ? a[2] : b[2], c ? a[3] : b[3]}; }
DI u32 enc24(float v, u32 tag8) { return ((asu(v) + 0x80u) & 0xFFFFFF00u) | tag8; }

#define LDW(off) __builtin_amdgcn_raw_buffer_load_b128(rs, (int)(off), 0, 16)
#define STW128(v, off) __builtin_amdgcn_raw_buffer_store_b128(v, rs, (int)(off), 0, 16)
#define STW32(v, off) __builtin_amdgcn_raw_buffer_store_b32(v, rs, (int)(off), 0, 16)
#define ADELAY 0
#define SLP 1
#define PAUSE() do { __builtin_amdgcn_s_sleep(SLP); CFENCE(); } while (0)

#define STAMP(k)
#define STAMP_FLUSH()

DI u32 bad8(v4u a, u32 t) { return ((a.x ^ t) | (a.y ^ t) | (a.z ^ t) | (a.w ^ t)) & 0xFFu; }
DI f4 dec24(v4u a) { return __builtin_bit_cast(f4, a & 0xFFFFFF00u); }
DI v4u enc24v(f4 r, u32 t) { return v4u{enc24(r[0], t), enc24(r[1], t), enc24(r[2], t), enc24(r[3], t)}; }
template <int NL, int STRIDE> DI void gather24(__amdgpu_buffer_rsrc_t rs, u32 off, u32 tag8, v4u (&a)[NL]) {
#pragma unroll
  for (int n = 0; n < NL; ++n) a[n] = LDW(off + n * STRIDE);
  for (int spin = 0; spin < SPIN_CAP; ++spin) {
    u32 bad = 0;
#pragma unroll
    for (int n = 0; n < NL; ++n) bad |= bad8(a[n], tag8);
    if (__all(bad == 0u)) break;
    PAUSE();
#pragma unroll
    for (int n = 0; n < NL; ++n) if (__any(bad8(a[n], tag8) != 0u)) a[n] = LDW(off + n * STRIDE);
  }
}
template <int NL> DI void gather16(__amdgpu_buffer_rsrc_t rs, const u32 (&off)[NL], u32 tag16, v4u (&a)[NL]) {
  for (int spin = 0; spin < SPIN_CAP; ++spin) {
    u32 bad = 0;
#pragma unroll
    for (int n = 0; n < NL; ++n) bad |= (a[n].w >> 16) ^ tag16;
    if (__all(bad == 0u)) break;
    PAUSE();
#pragma unroll
    for (int n = 0; n < NL; ++n) if (__any((a[n].w >> 16) != tag16)) a[n] = LDW(off[n]);
  }
}
template <int NL> DI f4 gather_reduce(__amdgpu_buffer_rsrc_t rs, u32 off, u32 tag8) {
  v4u a[NL];
  gather24<NL, 16 * 8192>(rs, off, tag8, a);
  f4 s = dec24(a[0]);
#pragma unroll
  for (int n = 1; n < NL; ++n) s += dec24(a[n]);
#pragma unroll
  for (int e = 0; e < 4; ++e) { float v = s[e]; v += ror<8>(v); v += ror<4>(v); v = sum16x(v); v = sum32x(v); s[e] = v; }
  return s;
}

__global__ void __launch_bounds__(512, 2)
fused_decode(const u16* __restrict__ x, const u16* __restrict__ ckv, const u16* __restrict__ wq, const u16* __restrict__ wuk,
             const u16* __restrict__ wuv, const u16* __restrict__ wo, const u16* __restrict__ wg, const u16* __restrict__ wu,
             const u16* __restrict__ wd, u16* __restrict__ out, unsigned char* ws) {
  extern __shared__ __attribute__((aligned(16))) unsigned char smem[];
  const u32 L = (u32)(uintptr_t)(LAS unsigned char*)smem;
  asm volatile("" ::"s"(x), "s"(ckv), "s"(wq), "s"(wuk), "s"(wuv), "s"(wo), "s"(wg), "s"(wu), "s"(wd), "s"(out), "s"(ws) : "memory");
  const u32 tid = threadIdx.x, lane = tid & 63;
  const u32 w = __builtin_amdgcn_readfirstlane(tid >> 6);
  const u32 bid = blockIdx.x, xcd = bid & 7, role = (bid >> 3) & 1, slot = bid >> 4, u = xcd * 16 + slot;
  const u32 i = lane & 15, g = lane >> 4, q = i >> 2, p = lane & 3;
  const __amdgpu_buffer_rsrc_t rs = __builtin_amdgcn_make_buffer_rsrc((void*)ws, 0, (int)WS_END, 0x00020000);
  const v4u sws = mk_srd(ws, WS_END);
  STAMP(0);
  unsigned long long pc;
  asm volatile("s_getpc_b64 %0" : "=s"(pc));
  const v4u scode = mk_srd((const void*)(pc & ~127ull), CODE_PF_BYTES);

  if (role == 0) {
    constexpr u32 QIMG = 131072, GU = 0, DN = 32768, PIMG = 65536, MX = 73728, LS = 74752, XN = 81920, RED = 90112, ACT = 94208, TR = 98304 - 8192;
    const u32 b = u >> 5;
    const u32 po_off = WS_PO + (((lane >> 3) * 4 + (w >> 1)) * 128 + u) * 640 + (w & 1) * 128 + (lane & 7) * 16;
    const u32 fx_off = WS_PO + (((lane >> 2) * 4 + (w >> 1)) * 128 + u) * 640 + 512 + (w & 1) * 64 + p * 16;
    if (w == 7 && !NOPF) {
#pragma unroll
      for (int n = 0; n < CODE_PF_LINES; ++n) dma4(scode, lane * 128, n * 8192, L + QIMG);
    }
    if (!NOTOUCH) dma4(sws, fx_off, 0, L + QIMG + 256);
    if (ADELAY) __builtin_amdgcn_s_sleep(ADELAY);
    {
      const v4u s = mk_srd(ckv, 4u * 4096 * 1024);
#pragma unroll
      for (int r = 0; r < 16; ++r) dma16(s, (lane ^ fsw(r)) * 16, (u * 128 + 16 * w + r) * 1024, L + (16 * w + r) * 1024);
    }
    const u32 E = __builtin_amdgcn_raw_buffer_load_b32(rs, WS_EPOCH, 0, 16);
    v4u qa[5];
    const u32 qa_off = WS_QA + b * 5 * 8192 + tid * 16;
#pragma unroll
    for (int n = 0; n < 5; ++n) qa[n] = LDW(qa_off + n * 8192);
    STAMP(13);
    VMCNT(6);
    STAMP(1);
    BAR();
    STAMP(2);
    v4u kf[16], vf[4][4];
    {
      const u32 kb = L + (16 * w + i) * 1024;
#pragma unroll
      for (int s = 0; s < 16; ++s) kf[s] = lds128(kb + (((4 * (s & 3) + g) ^ fsw(i)) * 16) + (s >> 2) * 256);
      const u32 fv = (q | ((g & 1) << 2)) << 1;
#pragma unroll
      for (int ct = 0; ct < 4; ++ct) {
        const u32 vb = L + (8 * g + q) * 1024 + (((8 * w + 2 * ct + (p >> 1)) ^ fv) * 16) + 8 * (p & 1);
#pragma unroll
        for (int ks = 0; ks < 4; ++ks) vf[ct][ks] = trfrag(vb + ks * 32768, vb + ks * 32768 + 4096);
      }
    }
    SCHED();
    const u32 tag32 = __builtin_amdgcn_readfirstlane(E) + 1u, tag16 = tag32 & 0xFFFFu, tag8 = tag32 & 0xFFu;
    {
      const u32 offs[5] = {qa_off, qa_off + 8192, qa_off + 2 * 8192, qa_off + 3 * 8192, qa_off + 4 * 8192};
      gather16<5>(rs, offs, tag16, qa);
    }
    STAMP(3);
    {
      qa[0].w = __builtin_amdgcn_perm(qa[4].x, qa[0].w, 0x05040100);
      qa[1].w = __builtin_amdgcn_perm(qa[4].x, qa[1].w, 0x07060100);
      qa[2].w = __builtin_amdgcn_perm(qa[4].y, qa[2].w, 0x05040100);
      qa[3].w = __builtin_amdgcn_perm(qa[4].y, qa[3].w, 0x07060100);
      const u32 hh = ((tid >> 6) << 2) | (((tid >> 2) & 1) << 1) | ((tid >> 3) & 1), jj = (tid >> 4) & 3;
#pragma unroll
      for (int n = 0; n < 4; ++n) st128(L + QIMG + hh * 1024 + (((16 * jj + 4 * n + p) ^ fsw(hh)) * 16), qa[n]);
    }
    BAR();
    STAMP(4);
    const bool ffn = slot < 14;
    const u32 fu = xcd * 14 + slot;
    f4 sc[2] = {{0.f, 0.f, 0.f, 0.f}, {0.f, 0.f, 0.f, 0.f}};
    {
      u32 qb[4];
#pragma unroll
      for (int s = 0; s < 4; ++s) qb[s] = L + QIMG + i * 1024 + (((4 * s + g) ^ fsw(i)) * 16);
      v4u qf[8];
#pragma unroll
      for (int s = 0; s < 4; ++s) { qf[2 * s] = lds128(qb[s]); qf[2 * s + 1] = lds128(qb[s] + 16384); }
      SCHED();
#pragma unroll
      for (int s = 0; s < 16; ++s) {
        sc[0] = mfma32(kf[s], qf[2 * (s & 3)], sc[0]);
        sc[1] = mfma32(kf[s], qf[2 * (s & 3) + 1], sc[1]);
        if (s < 12) { qf[2 * (s & 3)] = lds128(qb[s & 3] + ((s >> 2) + 1) * 256); qf[2 * (s & 3) + 1] = lds128(qb[s & 3] + 16384 + ((s >> 2) + 1) * 256); }
        SCHED();
      }
    }
#pragma unroll
    for (int ht = 0; ht < 2; ++ht) {
      st32f(L + MX + ((16 * ht + i) * 8 + w) * 4, max32x(max16x(mx4(sc[ht]))));
    }
    BAR();
    STAMP(5);
#pragma unroll
    for (int ht = 0; ht < 2; ++ht) {
      const f4 a = __builtin_bit_cast(f4, lds128(L + MX + (16 * ht + i) * 32)), c = __builtin_bit_cast(f4, lds128(L + MX + (16 * ht + i) * 32 + 16));
      const float m = mx(mx4(a), mx4(c));
      v2u pk;
      pk.x = pkbf(__builtin_amdgcn_exp2f(sc[ht][0] - m), __builtin_amdgcn_exp2f(sc[ht][1] - m));
      pk.y = pkbf(__builtin_amdgcn_exp2f(sc[ht][2] - m), __builtin_amdgcn_exp2f(sc[ht][3] - m));
      st64(L + PIMG + (16 * ht + i) * 256 + (((2 * w + (g >> 1)) ^ fsw(i)) * 16) + 8 * (g & 1), pk);
      float l = (bflo(pk.x) + bfhi(pk.x)) + (bflo(pk.y) + bfhi(pk.y));
      l = sum32x(sum16x(l));
      st32f(L + LS + ((16 * ht + i) * 8 + w) * 4, l);
    }
    BAR();
    STAMP(6);
    if (w == 0 && lane < 32) {
      const f4 a = __builtin_bit_cast(f4, lds128(L + MX + lane * 32)), c = __builtin_bit_cast(f4, lds128(L + MX + lane * 32 + 16));
      const f4 d = __builtin_bit_cast(f4, lds128(L + LS + lane * 32)), e = __builtin_bit_cast(f4, lds128(L + LS + lane * 32 + 16));
      const float m = mx(mx4(a), mx4(c));
      const float l = ((d[0] + d[1]) + (d[2] + d[3])) + ((e[0] + e[1]) + (e[2] + e[3]));
      STW128((v4u{asu(m), tag32, asu(l), tag32}), WS_ST + (lane * 128 + u) * 16);
    }
    f4 o[2][4];
#pragma unroll
    for (int ht = 0; ht < 2; ++ht)
#pragma unroll
      for (int ct = 0; ct < 4; ++ct) o[ht][ct] = f4{0.f, 0.f, 0.f, 0.f};
    {
      v4u pf[2][4];
#pragma unroll
      for (int ht = 0; ht < 2; ++ht)
#pragma unroll
        for (int ks = 0; ks < 4; ++ks) pf[ht][ks] = lds128(L + PIMG + (16 * ht + i) * 256 + (((4 * ks + g) ^ fsw(i)) * 16));
      SCHED();
      const u32 T = L + TR + w * 5120;
      u32 disp[4];
#pragma unroll
      for (int ht = 0; ht < 2; ++ht) {
#pragma unroll
        for (int ks = 0; ks < 4; ++ks)
#pragma unroll
          for (int ct = 0; ct < 4; ++ct) o[ht][ct] = mfma32(vf[ct][ks], pf[ht][ks], o[ht][ct]);
      }
#pragma unroll
      for (int ht = 0; ht < 2; ++ht)
#pragma unroll
        for (int tp = 0; tp < 2; ++tp) {
          const f4 a = o[ht][2 * tp], c = o[ht][2 * tp + 1];
          v4u v = {pkbf(a[0], a[1]), pkbf(a[2], a[3]), pkbf(c[0], c[1]), pkbf(c[2], c[3])};
          disp[2 * ht + tp] = v.w;
          v.w = (v.w & 0xFFFFu) | (tag16 << 16);
          st128(T + (16 * ht + i) * 128 + (((4 * tp + g) ^ (i & 7)) * 16), v);
        }
      st128(T + 4096 + i * 64 + ((g ^ ((i >> 1) & 3)) * 16),
            v4u{__builtin_amdgcn_perm(disp[1], disp[0], 0x07060302), __builtin_amdgcn_perm(disp[3], disp[2], 0x07060302), 0u, tag16 << 16});
      v4u t[5];
#pragma unroll
      for (int n = 0; n < 4; ++n) t[n] = lds128(T + n * 1024 + (lane >> 3) * 128 + (((lane & 7) ^ (lane >> 3)) * 16));
      t[4] = lds128(T + 4096 + (lane >> 2) * 64 + ((p ^ ((lane >> 3) & 3)) * 16));
#pragma unroll
      for (int n = 0; n < 4; ++n) STW128(t[n], po_off + (n & 1) * (8 * 4 * 128 * 640) + (n >> 1) * 256);
      STW128(t[4], fx_off);
    }
    STAMP(7);
    if (ffn) {
      BAR();
      const v4u s = mk_srd((w & 1) ? wu : wg, 512u * 1792 * 2);
      const u32 pos = lane >> 1, row = pos ^ (((pos >> 3) & 1) << 2);
#pragma unroll
      for (int n = 0; n < 4; ++n) dma16(s, row * 3584 + (lane & 1) * 16, ((w >> 1) * 128 + 32 * n) * 3584 + fu * 32, L + GU + w * 4096 + n * 1024);
      const v4u sd = mk_srd(wd, 1792u * 512 * 2);
      const u32 r8 = lane >> 3;
#pragma unroll
      for (int n = 0; n < 2; ++n) dma16(sd, r8 * 1024 + (((lane & 7) ^ ((r8 >> 1) & 1)) * 16), (fu * 16 + 8 * n) * 1024 + w * 128, L + DN + w * 2048 + n * 1024);
    }
    STAMP(14);
    if (ffn) {
      VMCNT(0);
      v4u gf[4];
      v2u df[4];
      {
        const u32 r0 = 8 * g + q, sw = (g & 1) << 2;
#pragma unroll
        for (int ks = 0; ks < 4; ++ks) {
          const u32 a = L + GU + w * 4096 + ks * 1024 + p * 8;
          gf[ks] = trfrag(a + ((r0) ^ sw) * 32, a + ((r0 + 4) ^ sw) * 32);
        }
        const u32 rd = 4 * g + q;
#pragma unroll
        for (int ct = 0; ct < 4; ++ct) df[ct] = tr64(L + DN + w * 2048 + rd * 128 + (((2 * p + (ct >> 1)) ^ ((rd >> 1) & 1)) * 16) + 8 * (ct & 1));
      }
      SCHED();
      STAMP(8);
      if (w < 4) {
        v4u hh[2];
        gather24<2, 1024>(rs, WS_H1 + w * 2048 + lane * 16, tag8, hh);
        STAMP(9);
        f4 fa = dec24(hh[0]), fc = dec24(hh[1]);
        float s = ((fa[0] + fa[1]) + (fa[2] + fa[3])) + ((fc[0] + fc[1]) + (fc[2] + fc[3]));
        s = sum32x(sum16x(rowsum16(s)));
        const float mean = s * (1.0f / 512.0f);
        fa -= mean; fc -= mean;
        float v = ((fa[0] * fa[0] + fa[1] * fa[1]) + (fa[2] * fa[2] + fa[3] * fa[3])) + ((fc[0] * fc[0] + fc[1] * fc[1]) + (fc[2] * fc[2] + fc[3] * fc[3]));
        v = sum32x(sum16x(rowsum16(v)));
        const float r = __builtin_amdgcn_rsqf(v * (1.0f / 512.0f) + 1e-5f);
        st64(L + XN + w * 1056 + lane * 8, v2u{pkbf(fa[0] * r, fa[1] * r), pkbf(fa[2] * r, fa[3] * r)});
        st64(L + XN + w * 1056 + 512 + lane * 8, v2u{pkbf(fc[0] * r, fc[1] * r), pkbf(fc[2] * r, fc[3] * r)});
      }
      BAR();
      f4 d = {0.f, 0.f, 0.f, 0.f};
      v4u xf[4];
#pragma unroll
      for (int ks = 0; ks < 4; ++ks) xf[ks] = lds128(L + XN + q * 1056 + ((w >> 1) * 128 + 32 * ks + 8 * g) * 2);
      SCHED();
#pragma unroll
      for (int ks = 0; ks < 4; ++ks) d = mfma32(gf[ks], xf[ks], d);
      st128(L + RED + w * 256 + q * 64 + g * 16, __builtin_bit_cast(v4u, d));
      BAR();
      if (w == 0) {
        const u32 a = L + RED + lane * 4;
        const float gg = (lds32f(a) + lds32f(a + 512)) + (lds32f(a + 1024) + lds32f(a + 1536));
        const float uu = (lds32f(a + 256) + lds32f(a + 768)) + (lds32f(a + 1280) + lds32f(a + 1792));
        const float act = gg * __builtin_amdgcn_rcpf(1.0f + __builtin_amdgcn_exp2f(gg * -1.4426950408889634f)) * uu;
        st16(L + ACT + lane * 2, (u16)(pkbf(act, act) & 0xFFFFu));
      }
      BAR();
      const v2u af = lds64(L + ACT + q * 32 + g * 8);
      f4 dd[4];
#pragma unroll
      for (int ct = 0; ct < 4; ++ct) dd[ct] = mfma16(df[ct], af, f4{0.f, 0.f, 0.f, 0.f});
      const f4 r = sel4(p & 2, sel4(p & 1, dd[3], dd[2]), sel4(p & 1, dd[1], dd[0]));
      STW128(enc24v(r, tag8), WS_FP + fu * 8192 + q * 2048 + (64 * w + 16 * g + 4 * p) * 4);
      STAMP(10);
    }
    STAMP_FLUSH();
  } else {
    constexpr u32 WQ = 0, WUK = 32768, X = 65536, RED = 69632, QAS = 73728, PF = 77824, QL = 79872, EP = 81920, WUV = 0, WO = 32768, OIMG = 0, CIMG = 4096;
    const u32 h = u >> 2, j = u & 3;
    const u32 ap_off = WS_AP + u * 8192 + q * 2048 + (64 * w + 16 * g + 4 * p) * 4;
    const u32 bb = w >> 1, sh = w & 1, w1 = (lane >> 3) & 1, tp = (lane >> 2) & 1;
    const u32 po_base = WS_PO + (((h & 15) * 4 + j) * 128 + bb * 32 + 16 * sh + g) * 640 + w1 * 64 + p * 16;
    if (w < 4) {
      dma16(mk_srd(x, 4096), lane * 16, w * 1024, L + X + w * 1024);
    } else if (w == 7) {
#pragma unroll
      for (int n = 0; n < CODE_PF_LINES; ++n) if (!NOPF) dma4(scode, lane * 128, n * 8192, L + PF);
    } else if (w == 4) {
      dma4c(sws, lane * 4, WS_EPOCH, L + EP);
    } else {
      if (!NOTOUCH) dma4(sws, w == 6 ? ap_off : po_base, 0, L + PF + 256);
    }
    const u32 vo0 = g * 8192 + ((i ^ (g << 1)) * 16), vo1 = g * 8192 + ((i ^ ((g | 4) << 1)) * 16);
    const u32 so = (128 * j + 16 * w) * 8192 + h * 256;
    {
      const v4u s = mk_srd(wq, 512u * 8192), s2 = mk_srd(wuk, 512u * 8192);
#pragma unroll
      for (int n = 0; n < 4; ++n) dma16(s, n < 2 ? vo0 : vo1, so + 4 * n * 8192, L + WQ + (16 * w + 4 * n) * 256);
#pragma unroll
      for (int n = 0; n < 4; ++n) dma16(s2, n < 2 ? vo0 : vo1, so + 4 * n * 8192, L + WUK + (16 * w + 4 * n) * 256);
    }
    STAMP(13);
    VMCNT(8);
    STAMP(14);
    BAR();
    STAMP(1);
    v4u xn[4];
    const v2u xres = lds64(L + X + (u >> 5) * 1024 + ((u & 31) * 16 + 4 * p) * 2);
    const u32 tag32 = __builtin_amdgcn_readfirstlane(asu(lds32f(L + EP))) + 1u, tag16 = tag32 & 0xFFFFu, tag8 = tag32 & 0xFFu;
    {
      float v[32];
#pragma unroll
      for (int n = 0; n < 4; ++n) {
        const v4u t = lds128(L + X + q * 1024 + (4 * g + p) * 64 + n * 16);
#pragma unroll
        for (int e = 0; e < 4; ++e) { v[8 * n + 2 * e] = bflo(t[e]); v[8 * n + 2 * e + 1] = bfhi(t[e]); }
      }
      float s = 0.f;
#pragma unroll
      for (int e = 0; e < 32; ++e) s += v[e];
      s += qp<0xB1>(s); s += qp<0x4E>(s); s = sum32x(sum16x(s));
      const float mean = s * (1.0f / 512.0f);
      float vv = 0.f;
#pragma unroll
      for (int e = 0; e < 32; ++e) { const float d = v[e] - mean; vv += d * d; }
      vv += qp<0xB1>(vv); vv += qp<0x4E>(vv); vv = sum32x(sum16x(vv));
      const float r = __builtin_amdgcn_rsqf(vv * (1.0f / 512.0f) + 1e-5f);
#pragma unroll
      for (int ks = 0; ks < 4; ++ks) {
        const v4u t = lds128(L + X + q * 1024 + (128 * j + 32 * ks + 8 * g) * 2);
#pragma unroll
        for (int e = 0; e < 4; ++e) xn[ks][e] = pkbf((bflo(t[e]) - mean) * r, (bfhi(t[e]) - mean) * r);
      }
    }
    SCHED();
    VMCNT(4);
    BAR();
    STAMP(2);
    const u32 tra = (8 * g + q) * 256 + (((2 * w + (p >> 1)) ^ ((q | ((g & 1) << 2)) << 1)) * 16) + 8 * (p & 1);
    {
      f4 d = {0.f, 0.f, 0.f, 0.f};
      v4u wf[4];
#pragma unroll
      for (int ks = 0; ks < 4; ++ks) wf[ks] = trfrag(L + WQ + tra + ks * 8192, L + WQ + tra + ks * 8192 + 1024);
      SCHED();
#pragma unroll
      for (int ks = 0; ks < 4; ++ks) d = mfma32(wf[ks], xn[ks], d);
      st128(L + RED + q * 512 + (16 * w + 4 * g) * 4, __builtin_bit_cast(v4u, d));
    }
    BAR();
    if (w < 2) STW128(enc24v(__builtin_bit_cast(f4, lds128(L + RED + tid * 16)), tag8), WS_Q + u * 2048 + tid * 16);
    STAMP(3);
    if (w == 2 || w == 3) {
      v4u a[4];
      const u32 o = (tid - 128) * 16;
      gather24<4, 2048>(rs, WS_Q + h * 8192 + o, tag8, a);
      const f4 s = (dec24(a[0]) + dec24(a[1])) + (dec24(a[2]) + dec24(a[3]));
      st64(L + QL + (o >> 9) * 272 + ((o & 511) >> 1), v2u{pkbf(s[0], s[1]), pkbf(s[2], s[3])});
    }
    if (w >= 2) VMCNT(0); else VMCNT(1);
    v4u kf[4];
#pragma unroll
    for (int s = 0; s < 4; ++s) kf[s] = lds128(L + WUK + (16 * w + i) * 256 + (((4 * s + g) ^ fsw(i)) * 16));
    BAR();
    STAMP(4);
    {
      f4 d = {0.f, 0.f, 0.f, 0.f};
      v4u qf[4];
#pragma unroll
      for (int s = 0; s < 4; ++s) qf[s] = lds128(L + QL + q * 272 + s * 64 + g * 16);
      SCHED();
#pragma unroll
      for (int s = 0; s < 4; ++s) d = mfma32(kf[s], qf[s], d);
      constexpr float SC = 0.08838834764831845f * 1.4426950408889634f;
      st64(L + QAS + q * 256 + (16 * w + 4 * g) * 2, v2u{pkbf(d[0] * SC, d[1] * SC), pkbf(d[2] * SC, d[3] * SC)});
    }
    BAR();
    {
      const u32 set = ((h >> 2) << 6) | (j << 4) | ((h & 1) << 3) | (((h >> 1) & 1) << 2) | p;
      if (w == 0) {
        v4u v = lds128(L + QAS + q * 256 + (4 * g + p) * 16);
        v.w = (v.w & 0xFFFFu) | (tag16 << 16);
        STW128(v, WS_QA + (q * 5 + g) * 8192 + set * 16);
      } else if (w == 1 && lane < 16) {
        const u32 a = L + QAS + q * 256 + p * 16 + 14;
        const v4u v = {lds16(a) | (lds16(a + 64) << 16), lds16(a + 128) | (lds16(a + 192) << 16), 0u, tag16 << 16};
        STW128(v, WS_QA + (q * 5 + 4) * 8192 + set * 16);
      }
    }
    BAR();
    STAMP(5);
    {
      const v4u s2 = mk_srd(wuv, 512u * 8192);
#pragma unroll
      for (int n = 0; n < 4; ++n) dma16(s2, n < 2 ? vo0 : vo1, so + 4 * n * 8192, L + WUV + (16 * w + 4 * n) * 256);
      const v4u s = mk_srd(wo, 4096u * 1024);
#pragma unroll
      for (int n = 0; n < 16; ++n)
        dma16(s, (lane >> 3) * 1024 + (((lane & 7) ^ (g & 1)) * 16), (h * 128 + 8 * n) * 1024 + w * 128, L + WO + w * 16384 + n * 1024);
    }
    VMCNT(16);
    BAR();
    v4u uv[4], of[4][4];
    {
#pragma unroll
      for (int ks = 0; ks < 4; ++ks) uv[ks] = trfrag(L + WUV + tra + ks * 8192, L + WUV + tra + ks * 8192 + 1024);
    }
    BAR();
    STAMP(6);
    VMCNT(0);
    {
#pragma unroll
      for (int ct = 0; ct < 4; ++ct) {
        const u32 a = L + WO + w * 16384 + (8 * g + q) * 128 + (((2 * p + (ct >> 1)) ^ ((q >> 1) & 1)) * 16) + 8 * (ct & 1);
#pragma unroll
        for (int ks = 0; ks < 4; ++ks) of[ct][ks] = trfrag(a + ks * 4096, a + ks * 4096 + 512);
      }
    }
    SCHED();
    STAMP(7);
    float wgt[4];
    {
      v4u a;
      const u32 off = WS_ST + (h * 128 + bb * 32 + (lane & 31)) * 16;
      for (int spin = 0;; ++spin) {
        a = LDW(off);
        if (__all(a.y == tag32 && a.w == tag32) || spin > SPIN_CAP) break;
        PAUSE();
      }
      STAMP(8);
      const float m = asf(a.x), M = max16x(rowmax16(m));
      const float e = __builtin_amdgcn_exp2f(m - M);
      const float Ls = sum16x(rowsum16(e * asf(a.z)));
      const float wv = e * __builtin_amdgcn_rcpf(Ls);
#pragma unroll
      for (int n = 0; n < 4; ++n) wgt[n] = __shfl(wv, 16 * sh + 4 * n + g);
    }
    {
      v4u oc[4], fx[4];
      const u32 ht = h >> 4;
      const u32 ooff = po_base + ht * 256 + w1 * 64 + tp * 64, foff = po_base + 512;
      if (PODELAY) __builtin_amdgcn_s_sleep(PODELAY);
      v4u po[8];
      u32 offs[8];
#pragma unroll
      for (int n = 0; n < 4; ++n) { offs[n] = ooff + n * 2560; offs[4 + n] = foff + n * 2560; }
#pragma unroll
      for (int n = 0; n < 8; ++n) po[n] = LDW(offs[n]);
      gather16<8>(rs, offs, tag16, po);
#pragma unroll
      for (int n = 0; n < 4; ++n) { oc[n] = po[n]; fx[n] = po[4 + n]; }
      STAMP(9);
      float acc[8];
#pragma unroll
      for (int e = 0; e < 8; ++e) acc[e] = 0.f;
#pragma unroll
      for (int n = 0; n < 4; ++n) {
        const u32 fd = ht ? fx[n].y : fx[n].x;
        const float e7 = tp ? bfhi(fd) : bflo(fd);
        acc[0] += wgt[n] * bflo(oc[n].x); acc[1] += wgt[n] * bfhi(oc[n].x);
        acc[2] += wgt[n] * bflo(oc[n].y); acc[3] += wgt[n] * bfhi(oc[n].y);
        acc[4] += wgt[n] * bflo(oc[n].z); acc[5] += wgt[n] * bfhi(oc[n].z);
        acc[6] += wgt[n] * bflo(oc[n].w); acc[7] += wgt[n] * e7;
      }
      float r4[4];
#pragma unroll
      for (int e = 0; e < 4; ++e) {
        auto r = __builtin_amdgcn_permlane32_swap(asu(acc[e]), asu(acc[e + 4]), false, false);
        r4[e] = asf(r[0]) + asf(r[1]);
      }
      const u32 row = 4 * bb + 2 * sh + (g & 1);
      st64(L + OIMG + row * 256 + (((8 * w1 + 4 * tp + 2 * (g >> 1) + (p >> 1)) ^ fsw(row)) * 16) + 8 * (p & 1), v2u{pkbf(r4[0], r4[1]), pkbf(r4[2], r4[3])});
    }
    BAR();
    {
      f4 d = {0.f, 0.f, 0.f, 0.f};
      v4u bf[4];
#pragma unroll
      for (int ks = 0; ks < 4; ++ks) bf[ks] = lds128(L + OIMG + i * 256 + (((4 * ks + g) ^ fsw(i)) * 16));
      SCHED();
#pragma unroll
      for (int ks = 0; ks < 4; ++ks) d = mfma32(uv[ks], bf[ks], d);
      st64(L + CIMG + i * 256 + (((2 * w + (g >> 1)) ^ fsw(i)) * 16) + 8 * (g & 1), v2u{pkbf(d[0], d[1]), pkbf(d[2], d[3])});
    }
    BAR();
    {
      f4 d[4];
#pragma unroll
      for (int ct = 0; ct < 4; ++ct) d[ct] = f4{0.f, 0.f, 0.f, 0.f};
      v4u cf[4];
#pragma unroll
      for (int ks = 0; ks < 4; ++ks) cf[ks] = lds128(L + CIMG + i * 256 + (((4 * ks + g) ^ fsw(i)) * 16));
      SCHED();
#pragma unroll
      for (int ks = 0; ks < 4; ++ks)
#pragma unroll
        for (int ct = 0; ct < 4; ++ct) d[ct] = mfma32(of[ct][ks], cf[ks], d[ct]);
      f4 lo, hi, r;
#pragma unroll
      for (int e = 0; e < 4; ++e) {
        lo[e] = ((p & 1) ? d[1][e] : d[0][e]) + qp<0xB1>((p & 1) ? d[0][e] : d[1][e]);
        hi[e] = ((p & 1) ? d[3][e] : d[2][e]) + qp<0xB1>((p & 1) ? d[2][e] : d[3][e]);
        r[e] = ((p & 2) ? hi[e] : lo[e]) + qp<0x4E>((p & 2) ? lo[e] : hi[e]);
      }
      STW128(enc24v(r, tag8), ap_off);
    }
    STAMP(10);
    if (w == 0) {
      const u32 col = (u >> 5) * 2048 + ((u & 31) * 16 + 4 * p) * 4;
      f4 h1 = gather_reduce<8>(rs, WS_AP + (lane >> 2) * 8192 + col, tag8);
      h1 += f4{bflo(xres.x), bfhi(xres.x), bflo(xres.y), bfhi(xres.y)};
      if (lane < 4) STW128(enc24v(h1, tag8), WS_H1 + col);
      STAMP(11);
      const f4 y = h1 + gather_reduce<7>(rs, WS_FP + (lane >> 2) * 8192 + col, tag8);
      if (lane < 4) *(v2u*)(out + (col >> 2)) = v2u{pkbf(y[0], y[1]), pkbf(y[2], y[3])};
      if (u == 0 && lane == 0) STW32(tag32, WS_EPOCH);
      STAMP(12);
    }
    STAMP_FLUSH();
  }
}

extern "C" void kernel_launch(void* const* d_in, const int* in_sizes, int n_in, void* d_out, int out_size, void* d_ws, size_t ws_size,
                              hipStream_t stream) {
  (void)hipFuncSetAttribute((const void*)fused_decode, hipFuncAttributeMaxDynamicSharedMemorySize, (int)LDS_BYTES);
  hipLaunchKernelGGL(fused_decode, dim3(256), dim3(512), LDS_BYTES, stream, (const u16*)d_in[0], (const u16*)d_in[1], (const u16*)d_in[2],
                     (const u16*)d_in[3], (const u16*)d_in[4], (const u16*)d_in[5], (const u16*)d_in[7], (const u16*)d_in[8],
                     (const u16*)d_in[9], (u16*)d_out, (unsigned char*)d_ws);
}
```

```cpp
#include <hip/hip_runtime.h>
#include <stdint.h>
#include <utility>

typedef unsigned u32;
typedef unsigned short u16;
typedef u32 v4u __attribute__((ext_vector_type(4)));
typedef u32 v2u __attribute__((ext_vector_type(2)));
typedef float f4 __attribute__((ext_vector_type(4)));
typedef float f2 __attribute__((ext_vector_type(2)));
typedef float f16v __attribute__((ext_vector_type(16)));
typedef short s8 __attribute__((ext_vector_type(8)));
typedef short s4 __attribute__((ext_vector_type(4)));
typedef __bf16 bf2 __attribute__((ext_vector_type(2)));
#define LAS __attribute__((address_space(3)))
#define DI __device__ __forceinline__

constexpr u32 WS_QA = 0;
constexpr u32 WS_ST = WS_QA + 163840;
constexpr u32 WS_H1 = WS_ST + 65536;
constexpr u32 WS_HINT = WS_H1 + 65536;
constexpr u32 WS_Q = WS_HINT + 4096;
constexpr u32 WS_AP = WS_Q + 262144;
constexpr u32 WS_FP = WS_AP + 1048576;
constexpr u32 WS_PO = WS_FP + 112 * 8192;
constexpr u32 WS_END = WS_PO + 32 * 4 * 128 * 256;
constexpr u32 SLOT = 8u << 20, SLOT0 = 4096;
#define NSLOT 8
static_assert(WS_END <= SLOT, "slot size");
constexpr u32 WS_STAMP = SLOT0 + NSLOT * SLOT;
constexpr int SPIN_CAP = 1 << 17;
constexpr u32 LDS_BYTES = 163840;
#define WIN 8
#define CODE_PF 7
#define FFN_E2 0
#define SLP 1
#define FFN_EARLY 2

enum { K_ADELAY, K_TQA, K_TWT, K_DPO, K_DAP, K_DH1, K_DFP, K_PATH, K_N };
constexpr int c_knob[K_N] = {22, 440, 420, 30, 30, 19, 37, 40};
#define KNOB(i) c_knob[i]
#define USE_HINT 1
#define FP_HINT 1
DI v4u mk_srd(const void* p, u32 bytes) {
  unsigned long long a = (unsigned long long)p;
  return v4u{(u32)a, (u32)(a >> 32) & 0xFFFFu, bytes, 0x00020000u};
}
DI void dma16(v4u srd, u32 voff, u32 soff, u32 lds) {
  u32 keep;
  asm volatile("s_mov_b32 %0, m0\n\ts_mov_b32 m0, %4\n\ts_nop 2\n\tbuffer_load_dwordx4 %1, %2, %3 offen nt lds\n\ts_mov_b32 m0, %0"
               : "=&s"(keep) : "v"(voff), "s"(srd), "s"(soff), "s"(lds) : "memory");
}
DI void dma16d(v4u srd, u32 voff, u32 soff, u32 lds) {
  u32 keep;
  asm volatile("s_mov_b32 %0, m0\n\ts_mov_b32 m0, %4\n\ts_nop 2\n\tbuffer_load_dwordx4 %1, %2, %3 offen lds\n\ts_mov_b32 m0, %0"
               : "=&s"(keep) : "v"(voff), "s"(srd), "s"(soff), "s"(lds) : "memory");
}
DI void dma4(v4u srd, u32 voff, u32 soff, u32 lds) {
  u32 keep;
  asm volatile("s_mov_b32 %0, m0\n\ts_mov_b32 m0, %4\n\ts_nop 2\n\tbuffer_load_dword %1, %2, %3 offen lds\n\ts_mov_b32 m0, %0"
               : "=&s"(keep) : "v"(voff), "s"(srd), "s"(soff), "s"(lds) : "memory");
}
template <int N> DI void vmcnt() { asm volatile("s_waitcnt vmcnt(%0)" ::"i"(N) : "memory"); }
#define VMCNT(n) asm volatile("s_waitcnt vmcnt(" #n ")" ::: "memory")
#define BAR() asm volatile("s_waitcnt lgkmcnt(0)\n\ts_barrier" ::: "memory")
#define CFENCE() asm volatile("" ::: "memory")
#define SCHED() __builtin_amdgcn_sched_barrier(0)
#define NOW() ((u32)__builtin_amdgcn_s_memrealtime() - t_start)
#define UNTIL(t) do { const u32 t_ = (t); while (NOW() < t_) __builtin_amdgcn_s_sleep(1); } while (0)
#define SLEEP(n) do { const int n_ = (n); _Pragma("nounroll") for (int z_ = 0; z_ < n_; ++z_) __builtin_amdgcn_s_sleep(1); } while (0)

DI v4u lds128(u32 a) { return *(LAS v4u*)(uintptr_t)a; }
DI f4 lds128f(u32 a) { return *(LAS f4*)(uintptr_t)a; }
DI v2u lds64(u32 a) { return *(LAS v2u*)(uintptr_t)a; }
DI float lds32f(u32 a) { return *(LAS float*)(uintptr_t)a; }
DI u32 lds16(u32 a) { return *(LAS u16*)(uintptr_t)a; }
DI void st128(u32 a, v4u v) { *(LAS v4u*)(uintptr_t)a = v; }
DI void st64(u32 a, v2u v) { *(LAS v2u*)(uintptr_t)a = v; }
DI void st32f(u32 a, float v) { *(LAS float*)(uintptr_t)a = v; }
DI void st16(u32 a, u16 v) { *(LAS u16*)(uintptr_t)a = v; }
DI v2u tr64(u32 a) { return __builtin_bit_cast(v2u, __builtin_amdgcn_ds_read_tr16_b64_v4i16((LAS s4*)(uintptr_t)a)); }
DI v4u trfrag(u32 a0, u32 a1) { v2u x = tr64(a0), y = tr64(a1); return v4u{x.x, x.y, y.x, y.y}; }

DI float asf(u32 v) { return __builtin_bit_cast(float, v); }
DI u32 asu(float v) { return __builtin_bit_cast(u32, v); }
DI float bflo(u32 v) { return asf(v << 16); }
DI float bfhi(u32 v) { return asf(v & 0xFFFF0000u); }
DI u32 pkbf(float a, float b) { f2 v = {a, b}; return __builtin_bit_cast(u32, __builtin_convertvector(v, bf2)); }
DI v2u pk4(f4 a) { return v2u{pkbf(a[0], a[1]), pkbf(a[2], a[3])}; }
DI f4 mfma32(v4u a, v4u b, f4 c) { return __builtin_amdgcn_mfma_f32_16x16x32_bf16(__builtin_bit_cast(s8, a), __builtin_bit_cast(s8, b), c, 0, 0, 0); }
DI f4 mfma16(v2u a, v2u b, f4 c) { return __builtin_amdgcn_mfma_f32_16x16x16bf16_1k(__builtin_bit_cast(s4, a), __builtin_bit_cast(s4, b), c, 0, 0, 0); }
DI f16v mfmaL(v4u a, v4u b, f16v c) { return __builtin_amdgcn_mfma_f32_32x32x16_bf16(__builtin_bit_cast(s8, a), __builtin_bit_cast(s8, b), c, 0, 0, 0); }
template <int N> DI float ror(float v) { return asf((u32)__builtin_amdgcn_update_dpp(0, (int)asu(v), 0x120 + N, 0xf, 0xf, false)); }
template <int C> DI float qp(float v) { return asf((u32)__builtin_amdgcn_update_dpp(0, (int)asu(v), C, 0xf, 0xf, false)); }
DI float mx(float a, float b) { return __builtin_elementwise_maximum(a, b); }
DI float mx4(f4 a) { return mx(mx(a[0], a[1]), mx(a[2], a[3])); }
DI float sum4(f4 a) { return (a[0] + a[1]) + (a[2] + a[3]); }
DI float sum16x(float v) { auto r = __builtin_amdgcn_permlane16_swap(asu(v), asu(v), false, false); return asf(r[0]) + asf(r[1]); }
DI float sum32x(float v) { auto r = __builtin_amdgcn_permlane32_swap(asu(v), asu(v), false, false); return asf(r[0]) + asf(r[1]); }
DI float max16x(float v) { auto r = __builtin_amdgcn_permlane16_swap(asu(v), asu(v), false, false); return mx(asf(r[0]), asf(r[1])); }
DI float max32x(float v) { auto r = __builtin_amdgcn_permlane32_swap(asu(v), asu(v), false, false); return mx(asf(r[0]), asf(r[1])); }
DI float rowsum16(float v) { v += ror<8>(v); v += ror<4>(v); v += ror<2>(v); v += ror<1>(v); return v; }
DI float rowmax16(float v) { v = mx(v, ror<8>(v)); v = mx(v, ror<4>(v)); v = mx(v, ror<2>(v)); v = mx(v, ror<1>(v)); return v; }
DI u32 fsw(u32 row) { return ((row & 3u) | (((row >> 3) & 1u) << 2)) << 1; }
DI u32 fsw5(u32 row) { return fsw(row) | ((row >> 4) & 1u); }
DI f4 sel4(bool c, f4 a, f4 b) { return f4{c ? a[0] : b[0], c ? a[1] : b[1], c ? a[2] : b[2], c ? a[3] : b[3]}; }
DI u32 enc24(float v, u32 t) { return ((asu(v) + 0x80u) & 0xFFFFFF00u) | t; }
DI v4u enc24v(f4 r, u32 t) { return v4u{enc24(r[0], t), enc24(r[1], t), enc24(r[2], t), enc24(r[3], t)}; }

#define LDW(off) __builtin_amdgcn_raw_buffer_load_b128(rs, (int)(off), 0, 16)
#define STW128(v, off) __builtin_amdgcn_raw_buffer_store_b128(v, rs, (int)(off), 0, 16)
#define PAUSE() do { __builtin_amdgcn_s_sleep(SLP); CFENCE(); } while (0)

#define STAMP(k)
#define STAMP_FLUSH()
#define COUNT(k, v)

template <int... Is, class F> DI void sfor_(std::integer_sequence<int, Is...>, F f) { (f(std::integral_constant<int, Is>{}), ...); }
template <int N, class F> DI void sfor(F f) { sfor_(std::make_integer_sequence<int, N>{}, f); }
template <int N> struct Regs { v4u h; Regs<N - 1> t; template <int I> DI v4u& at() { if constexpr (I == 0) return h; else return t.template at<I - 1>(); } };
template <> struct Regs<1> { v4u h; template <int I> DI v4u& at() { return h; } };
#define AT(a, n) (a).template at<(n).value>()
template <int NL, int STRIDE, bool ISSUED = false> DI int gather(__amdgpu_buffer_rsrc_t rs, u32 off, v4u key, v4u mask, Regs<NL>& a) {
  if (!ISSUED) sfor<NL>([&](auto n) { AT(a, n) = LDW(off + n.value * STRIDE); });
  u32 pend = 0;
  sfor<NL>([&](auto n) {
    AT(a, n) ^= key;
    const v4u t = AT(a, n) & mask;
    if (!__all((t.x | t.y | t.z | t.w) == 0u)) pend |= 1u << n.value;
  });
  int spin = 0;
  while (pend != 0u && spin < SPIN_CAP) {
    ++spin;
    PAUSE();
    sfor<NL>([&](auto n) { if (pend >> n.value & 1u) AT(a, n) = LDW(off + n.value * STRIDE); });
    sfor<NL>([&](auto n) {
      if (pend >> n.value & 1u) {
        AT(a, n) ^= key;
        const v4u t = AT(a, n) & mask;
        if (__all((t.x | t.y | t.z | t.w) == 0u)) pend &= ~(1u << n.value);
      }
    });
  }
  return spin;
}
template <int NL> DI f4 gather_reduce(__amdgpu_buffer_rsrc_t rs, u32 off, u32 tag8, int& spins) {
  Regs<NL> a;
  spins = gather<NL, 16 * 8192>(rs, off, v4u{tag8, tag8, tag8, tag8}, v4u{0xFFu, 0xFFu, 0xFFu, 0xFFu}, a);
  f4 s = __builtin_bit_cast(f4, a.h);
  sfor<NL - 1>([&](auto n) { s += __builtin_bit_cast(f4, a.template at<n.value + 1>()); });
#pragma unroll
  for (int e = 0; e < 4; ++e) { float v = s[e]; v += ror<8>(v); v += ror<4>(v); v = sum16x(v); v = sum32x(v); s[e] = v; }
  return s;
}

__global__ void __launch_bounds__(512, 2)
fused_decode(const u16* __restrict__ x, const u16* __restrict__ ckv, const u16* __restrict__ wq, const u16* __restrict__ wuk,
             const u16* __restrict__ wuv, const u16* __restrict__ wo, const u16* __restrict__ wg, const u16* __restrict__ wu,
             const u16* __restrict__ wd, u16* __restrict__ out, unsigned char* ws) {
  extern __shared__ __attribute__((aligned(16))) unsigned char smem[];
  const u32 L = (u32)(uintptr_t)(LAS unsigned char*)smem;
  asm volatile("" ::"s"(x), "s"(ckv), "s"(wq), "s"(wuk), "s"(wuv), "s"(wo), "s"(wg), "s"(wu), "s"(wd), "s"(out), "s"(ws) : "memory");
  const u32 tid = threadIdx.x, lane = tid & 63;
  const u32 w = __builtin_amdgcn_readfirstlane(tid >> 6);
#define SLOTX 0
  const u32 bid = blockIdx.x, xcd = bid & 7, role = (bid >> 3) & 1, slot = (bid >> 4) ^ SLOTX, u = xcd * 16 + slot;
  const u32 i = lane & 15, g = lane >> 4, q = i >> 2, p = lane & 3;
  STAMP(0);
  int spins = 0;
  const u32 t_start = (u32)__builtin_amdgcn_s_memrealtime();
  if (CODE_PF && w == 6) {
    unsigned long long pc;
    asm volatile("s_getpc_b64 %0" : "=s"(pc));
    dma4(mk_srd((const void*)((pc & ~127ull) + (bid >> 3) * (CODE_PF * 128)), CODE_PF * 128), lane * 128, 0, L + 96 * 1024);
  }
  const u32 Ev = *(const __attribute__((address_space(4))) u32*)(uintptr_t)ws;
#define EPOCH_SETUP()                                                                                                              \
  const u32 tag32 = Ev + 1u, tag16 = tag32 & 0xFFFFu, tag8 = tag32 & 0xFFu;                                                         \
  const __amdgpu_buffer_rsrc_t rs = __builtin_amdgcn_make_buffer_rsrc((void*)(ws + SLOT0 + (size_t)(tag32 % NSLOT) * SLOT), 0, (int)WS_END, 0x00020000); \
  const u32 tb0 = (tag32 & 1u) | ((tag32 & 2u) << 15), tb1 = ((tag32 >> 2) & 1u) | ((tag32 & 8u) << 13);                           \
  const v4u key8 = {tag8, tag8, tag8, tag8}, msk8 = {0xFFu, 0xFFu, 0xFFu, 0xFFu};

  if (role == 0) {
    constexpr u32 QIMG = 131072, DN = 32768, PIMG = 65536, MX = 73728, LS = 74752, XN = 81920, RED = 90112, ACT = 94208, TR = 98304;
    const u32 b = u >> 5, h32 = lane & 31, hf = lane >> 5;
    const bool ffn = slot < 14;
    const u32 fu = xcd * 14 + slot;
    const u32 GU = w * 4096;
    const v4u sgu = mk_srd((w & 1) ? wu : wg, 512u * 1792 * 2);
    const u32 gu_vo = ((lane >> 1) ^ ((((lane >> 1) >> 3) & 1) << 2)) * 3584 + (lane & 1) * 16;
#define GU_DMA(n) dma16(sgu, gu_vo, ((w >> 1) * 128 + 32 * (n)) * 3584 + fu * 32, L + GU + (n) * 1024)
    EPOCH_SETUP();
    SLEEP(KNOB(K_ADELAY));
    {
      const v4u s = mk_srd(ckv, 4u * 4096 * 1024);
#pragma unroll
      for (int r = 0; r < 16; ++r) {
        if (r >= WIN) vmcnt<(WIN < 16 ? WIN - 1 : 0)>();
        dma16(s, (lane ^ (fsw(r) | (w & 1))) * 16, (u * 128 + 16 * w + r) * 1024, L + (16 * w + r) * 1024);
      }
    }
    STAMP(13);
    Regs<5> qa;
    const u32 qa_off = WS_QA + b * 5 * 8192 + tid * 16;
    v4u fr[32];
    const bool late = NOW() + KNOB(K_PATH) > (u32)KNOB(K_TQA);
    if (late) {
      UNTIL(KNOB(K_TQA));
      sfor<5>([&](auto n) { AT(qa, n) = LDW(qa_off + n.value * 8192); });
      VMCNT(5);
    } else {
      VMCNT(0);
    }
    STAMP(1);
    BAR();
    if (w < 4) {
      const u32 kb = L + (32 * w + h32) * 1024;
#pragma unroll
      for (int s = 0; s < 32; ++s) fr[s] = lds128(kb + (((2 * (s & 7) + hf) ^ fsw5(h32)) * 16) + (s >> 3) * 256);
    } else {
      const u32 fv = ((q | ((g & 1) << 2)) << 1) | (g >> 1);
#pragma unroll
      for (int ct = 0; ct < 8; ++ct) {
        const u32 vb = L + (8 * g + q) * 1024 + (w - 4) * 256 + (((2 * ct + (p >> 1)) ^ fv) * 16) + 8 * (p & 1);
#pragma unroll
        for (int ks = 0; ks < 4; ++ks) fr[4 * ct + ks] = trfrag(vb + ks * 32768, vb + ks * 32768 + 4096);
      }
    }
    SCHED();
    if (!late) {
      UNTIL(KNOB(K_TQA));
      sfor<5>([&](auto n) { AT(qa, n) = LDW(qa_off + n.value * 8192); });
    }
    STAMP(2);
    u32 qb[8];
#pragma unroll
    for (int s = 0; s < 8; ++s) qb[s] = L + QIMG + h32 * 1024 + (((2 * s + hf) ^ fsw5(h32)) * 16);
    const u32 hh = (w << 2) | (((lane >> 2) & 1) << 1) | ((lane >> 3) & 1);
    u32 qw[4];
#pragma unroll
    for (int n = 0; n < 4; ++n) qw[n] = L + QIMG + hh * 1024 + (((16 * g + 4 * n + p) ^ fsw5(hh)) * 16);
    SCHED();
    STAMP(14);
    spins = gather<5, 8192, true>(rs, qa_off, v4u{0u, 0u, 0u, tag16 << 16}, v4u{0u, 0u, 0u, 0xFFFF0000u}, qa);
    COUNT(0, spins);
    STAMP(3);
    {
      const v4u fx = qa.at<4>();
      qa.at<0>().w = __builtin_amdgcn_perm(fx.x, qa.at<0>().w, 0x05040100);
      qa.at<1>().w = __builtin_amdgcn_perm(fx.x, qa.at<1>().w, 0x07060100);
      qa.at<2>().w = __builtin_amdgcn_perm(fx.y, qa.at<2>().w, 0x05040100);
      qa.at<3>().w = __builtin_amdgcn_perm(fx.y, qa.at<3>().w, 0x07060100);
      sfor<4>([&](auto n) { st128(qw[n.value], AT(qa, n)); });
    }
    BAR();
    STAMP(4);
    f16v sc;
    if (w < 4) {
#pragma unroll
      for (int e = 0; e < 16; ++e) sc[e] = 0.f;
      v4u qf[8];
#pragma unroll
      for (int s = 0; s < 8; ++s) qf[s] = lds128(qb[s]);
      SCHED();
#pragma unroll
      for (int s = 0; s < 32; ++s) {
        sc = mfmaL(fr[s], qf[s & 7], sc);
        if (s < 24) qf[s & 7] = lds128(qb[s & 7] + ((s >> 3) + 1) * 256);
        SCHED();
      }
      float m = mx(mx(mx(sc[0], sc[1]), mx(sc[2], sc[3])), mx(mx(sc[4], sc[5]), mx(sc[6], sc[7])));
      m = mx(m, mx(mx(mx(sc[8], sc[9]), mx(sc[10], sc[11])), mx(mx(sc[12], sc[13]), mx(sc[14], sc[15]))));
      st32f(L + MX + h32 * 16 + w * 4, max32x(m));
    } else if (ffn) {
#pragma unroll
      for (int n = 0; n < FFN_EARLY; ++n) GU_DMA(n);
    }
    BAR();
    STAMP(5);
    if (USE_HINT && w == 4 && slot == 0 && lane == 0) __builtin_amdgcn_raw_buffer_store_b32(tag32, rs, WS_HINT + 2048 + xcd * 256, 0, 16);
    if (w < 4) {
      const float m = mx4(lds128f(L + MX + h32 * 16));
      float l = 0.f;
#pragma unroll
      for (int rg = 0; rg < 4; ++rg) {
        f4 e;
#pragma unroll
        for (int r = 0; r < 4; ++r) e[r] = __builtin_amdgcn_exp2f(sc[4 * rg + r] - m);
        st64(L + PIMG + h32 * 256 + (((4 * w + rg) ^ fsw(h32)) * 16) + 8 * hf, pk4(e));
        l += sum4(e);
      }
      st32f(L + LS + h32 * 16 + w * 4, sum32x(l));
    }
    BAR();
    STAMP(6);
    if (w == 0 && lane < 32)
      STW128((v4u{asu(mx4(lds128f(L + MX + lane * 16))), tag32, asu(sum4(lds128f(L + LS + lane * 16))), tag32}), WS_ST + u * 512 + lane * 16);
    if (w < 4 && ffn) {
#pragma unroll
      for (int n = 0; n < FFN_E2; ++n) GU_DMA(n);
    }
    if (w >= 4) {
      v4u pf[2][4];
#pragma unroll
      for (int ht = 0; ht < 2; ++ht)
#pragma unroll
        for (int ks = 0; ks < 4; ++ks) pf[ht][ks] = lds128(L + PIMG + (16 * ht + i) * 256 + (((4 * ks + g) ^ fsw(i)) * 16));
      SCHED();
      const u32 T = L + TR + (w - 4) * 8192;
      const u32 po_off = WS_PO + ((g * 4 + (w - 4)) * 128 + u) * 256 + i * 16;
      f4 o[2][8];
#pragma unroll
      for (int ct = 0; ct < 8; ++ct) { o[0][ct] = f4{0.f, 0.f, 0.f, 0.f}; o[1][ct] = f4{0.f, 0.f, 0.f, 0.f}; }
#pragma unroll
      for (int ks = 0; ks < 4; ++ks)
#pragma unroll
        for (int ct = 0; ct < 8; ++ct) o[0][ct] = mfma32(fr[4 * ct + ks], pf[0][ks], o[0][ct]);
      SCHED();
#define PO_PACK(ht, tp)                                                                                                    \
  do {                                                                                                                     \
    const v2u a_ = pk4(o[ht][2 * (tp)]), c_ = pk4(o[ht][2 * (tp) + 1]);                                                     \
    st128(T + (16 * (ht) + i) * 256 + (((4 * (tp) + g) ^ (i & 7)) * 16),                                                    \
          v4u{(a_.x & 0xFFFEFFFEu) | tb0, (a_.y & 0xFFFEFFFEu) | tb1, (c_.x & 0xFFFEFFFEu) | tb0, (c_.y & 0xFFFEFFFEu) | tb1}); \
  } while (0)
#define PO_READ(n) lds128(T + (4 * (n) + g) * 256 + ((i ^ ((4 * (n) + g) & 7)) * 16))
      v4u t[4];
#pragma unroll
      for (int ks = 0; ks < 4; ++ks) {
#pragma unroll
        for (int ct = 0; ct < 8; ++ct) o[1][ct] = mfma32(fr[4 * ct + ks], pf[1][ks], o[1][ct]);
        if (ks == 0) { PO_PACK(0, 0); PO_PACK(0, 1); }
        if (ks == 1) { PO_PACK(0, 2); PO_PACK(0, 3); }
        if (ks == 2) {
#pragma unroll
          for (int n = 0; n < 4; ++n) t[n] = PO_READ(n);
        }
        if (ks == 3) {
#pragma unroll
          for (int n = 0; n < 4; ++n) STW128(t[n], po_off + n * (4 * 4 * 128 * 256));
        }
        SCHED();
      }
      PO_PACK(1, 0); PO_PACK(1, 1); PO_PACK(1, 2); PO_PACK(1, 3);
#pragma unroll
      for (int n = 0; n < 4; ++n) t[n] = PO_READ(4 + n);
#pragma unroll
      for (int n = 0; n < 4; ++n) STW128(t[n], po_off + (4 + n) * (4 * 4 * 128 * 256));
    }
    STAMP(7);
    if (ffn) {
      BAR();
#pragma unroll
      for (int n = 0; n < 4; ++n) if (n >= (w < 4 ? FFN_E2 : FFN_EARLY)) GU_DMA(n);
      {
        const v4u sd = mk_srd(wd, 1792u * 512 * 2);
        const u32 r8 = lane >> 3;
#pragma unroll
        for (int n = 0; n < 2; ++n) dma16(sd, r8 * 1024 + (((lane & 7) ^ ((r8 >> 1) & 1)) * 16), (fu * 16 + 8 * n) * 1024 + w * 128, L + DN + w * 2048 + n * 1024);
      }
      VMCNT(0);
      v4u gf[4];
      v2u df[4];
      {
        const u32 r0 = 8 * g + q, sw = (g & 1) << 2;
#pragma unroll
        for (int ks = 0; ks < 4; ++ks) {
          const u32 a = L + GU + ks * 1024 + p * 8;
          gf[ks] = trfrag(a + ((r0) ^ sw) * 32, a + ((r0 + 4) ^ sw) * 32);
        }
        const u32 rd = 4 * g + q;
#pragma unroll
        for (int ct = 0; ct < 4; ++ct) df[ct] = tr64(L + DN + w * 2048 + rd * 128 + (((2 * p + (ct >> 1)) ^ ((rd >> 1) & 1)) * 16) + 8 * (ct & 1));
      }
      SCHED();
      STAMP(8);
      if (w < 4) {
        if (USE_HINT) {
          for (int spin = 0; spin < SPIN_CAP; ++spin) {
            if (__builtin_amdgcn_raw_buffer_load_b32(rs, WS_HINT + xcd * 256, 0, 16) == tag32) break;
            PAUSE();
          }
          STAMP(11);
          SLEEP(KNOB(K_DH1));
        }
        Regs<2> hh;
        spins = gather<2, 1024>(rs, WS_H1 + xcd * 8192 + w * 2048 + lane * 16, key8, msk8, hh);
        COUNT(1, spins);
        STAMP(9);
        f4 fa = __builtin_bit_cast(f4, hh.at<0>()), fc = __builtin_bit_cast(f4, hh.at<1>());
        const float mean = sum32x(sum16x(rowsum16(sum4(fa) + sum4(fc)))) * (1.0f / 512.0f);
        fa -= mean; fc -= mean;
        const float v = sum32x(sum16x(rowsum16(sum4(fa * fa) + sum4(fc * fc))));
        const float r = __builtin_amdgcn_rsqf(v * (1.0f / 512.0f) + 1e-5f);
        st64(L + XN + w * 1056 + lane * 8, pk4(fa * r));
        st64(L + XN + w * 1056 + 512 + lane * 8, pk4(fc * r));
      }
      BAR();
      f4 d = {0.f, 0.f, 0.f, 0.f};
      v4u xf[4];
#pragma unroll
      for (int ks = 0; ks < 4; ++ks) xf[ks] = lds128(L + XN + q * 1056 + ((w >> 1) * 128 + 32 * ks + 8 * g) * 2);
      SCHED();
#pragma unroll
      for (int ks = 0; ks < 4; ++ks) d = mfma32(gf[ks], xf[ks], d);
      st128(L + RED + w * 256 + q * 64 + g * 16, __builtin_bit_cast(v4u, d));
      BAR();
      if (w == 0) {
        const u32 a = L + RED + lane * 4;
        const float gg = (lds32f(a) + lds32f(a + 512)) + (lds32f(a + 1024) + lds32f(a + 1536));
        const float uu = (lds32f(a + 256) + lds32f(a + 768)) + (lds32f(a + 1280) + lds32f(a + 1792));
        const float act = gg * __builtin_amdgcn_rcpf(1.0f + __builtin_amdgcn_exp2f(gg * -1.4426950408889634f)) * uu;
        st16(L + ACT + lane * 2, (u16)(pkbf(act, act) & 0xFFFFu));
      }
      BAR();
      const v2u af = lds64(L + ACT + q * 32 + g * 8);
      f4 dd[4];
#pragma unroll
      for (int ct = 0; ct < 4; ++ct) dd[ct] = mfma16(df[ct], af, f4{0.f, 0.f, 0.f, 0.f});
      const f4 r = sel4(p & 2, sel4(p & 1, dd[3], dd[2]), sel4(p & 1, dd[1], dd[0]));
      STW128(enc24v(r, tag8), WS_FP + fu * 8192 + q * 2048 + (64 * w + 16 * g + 4 * p) * 4);
      STAMP(10);
    }
    STAMP_FLUSH();
  } else {
    constexpr u32 WQ = 0, WUK = 131072, X = WUK + 28672, QAS = 0, WUV = 0, WO = 32768, OIMG = 0, CIMG = 4096;
    const u32 h = u >> 2, j = u & 3;
    const u32 vo0 = g * 8192 + ((i ^ (g << 1)) * 16), vo1 = g * 8192 + ((i ^ ((g | 4) << 1)) * 16);
    const u32 so = (128 * j + 16 * w) * 8192 + h * 256;
    v4u kf[4];
    if (w == 0) {
#pragma unroll
      for (int n = 0; n < 4; ++n) dma16d(mk_srd(x, 4096), lane * 16, n * 1024, L + X + n * 1024);
    }
    if (w < 7) {
      const v4u s2 = mk_srd(wuk, 512u * 8192);
#pragma unroll
      for (int n = 0; n < 4; ++n) dma16(s2, n < 2 ? vo0 : vo1, so + 4 * n * 8192, L + WUK + (16 * w + 4 * n) * 256);
    } else {
#pragma unroll
      for (int s = 0; s < 4; ++s) kf[s] = __builtin_nontemporal_load((const v4u*)(wuk + (size_t)(128 * j + 112 + i) * 4096 + h * 128 + 32 * s + 8 * g));
    }
    const v4u swq = mk_srd(wq, 512u * 8192);
#define WQ_DMA(qd)                                                                                                              \
  do {                                                                                                                          \
    const u32 r0_ = 128 * (((qd) + j) & 3) + 16 * w;                                                                            \
    _Pragma("unroll") for (int n = 0; n < 4; ++n) dma16(swq, n < 2 ? vo0 : vo1, (r0_ + 4 * n) * 8192 + h * 256, L + WQ + (r0_ + 4 * n) * 256); \
  } while (0)
    WQ_DMA(0);
    asm volatile("s_barrier" ::: "memory");
    v2u xres = {0u, 0u};
    if (w == 0) {
      VMCNT(8);
      STAMP(1);
      xres = lds64(L + X + (u >> 5) * 1024 + ((u & 31) * 16 + 4 * p) * 2);
      f4 v[8];
#pragma unroll
      for (int n = 0; n < 4; ++n) {
        const v4u t = lds128(L + X + q * 1024 + (4 * g + p) * 64 + n * 16);
        v[2 * n] = f4{bflo(t.x), bfhi(t.x), bflo(t.y), bfhi(t.y)};
        v[2 * n + 1] = f4{bflo(t.z), bfhi(t.z), bflo(t.w), bfhi(t.w)};
      }
      float s = sum4(((v[0] + v[1]) + (v[2] + v[3])) + ((v[4] + v[5]) + (v[6] + v[7])));
      s += qp<0xB1>(s); s += qp<0x4E>(s); s = sum32x(sum16x(s));
      const float mean = s * (1.0f / 512.0f);
#pragma unroll
      for (int n = 0; n < 8; ++n) v[n] -= mean;
      float vv = sum4(((v[0] * v[0] + v[1] * v[1]) + (v[2] * v[2] + v[3] * v[3])) + ((v[4] * v[4] + v[5] * v[5]) + (v[6] * v[6] + v[7] * v[7])));
      vv += qp<0xB1>(vv); vv += qp<0x4E>(vv); vv = sum32x(sum16x(vv));
      const float r = __builtin_amdgcn_rsqf(vv * (1.0f / 512.0f) + 1e-5f);
#pragma unroll
      for (int n = 0; n < 4; ++n) {
        const v2u a = pk4(v[2 * n] * r), c = pk4(v[2 * n + 1] * r);
        st128(L + X + q * 1024 + (4 * g + p) * 64 + n * 16, v4u{a.x, a.y, c.x, c.y});
      }
      STAMP(14);
    }
    WQ_DMA(1);
    BAR();
    EPOCH_SETUP();
    const u32 tra = (8 * g + q) * 256 + (((2 * w + (p >> 1)) ^ ((q | ((g & 1) << 2)) << 1)) * 16) + 8 * (p & 1);
    {
      v4u xn[16];
#pragma unroll
      for (int ks = 0; ks < 16; ++ks) xn[ks] = lds128(L + X + q * 1024 + ((ks * 64 + j * 256) & 1023) + g * 16);
      SCHED();
      WQ_DMA(2);
      f4 d0 = {0.f, 0.f, 0.f, 0.f}, d1 = {0.f, 0.f, 0.f, 0.f};
      sfor<4>([&](auto qd) {
        vmcnt<(qd.value == 0 ? 8 : 12 - 4 * qd.value)>();
        BAR();
        if (qd.value == 3) { STAMP(2); }
        const u32 a = L + WQ + tra + ((qd.value + j) & 3) * 32768;
        v4u wf[4];
#pragma unroll
        for (int ks = 0; ks < 4; ++ks) wf[ks] = trfrag(a + ks * 8192, a + ks * 8192 + 1024);
        if (qd.value == 3 && w < 7) {
#pragma unroll
          for (int s = 0; s < 4; ++s) kf[s] = lds128(L + WUK + (16 * w + i) * 256 + (((4 * s + g) ^ fsw(i)) * 16));
        }
        SCHED();
        d0 = mfma32(wf[0], xn[4 * qd.value], d0); d1 = mfma32(wf[1], xn[4 * qd.value + 1], d1);
        d0 = mfma32(wf[2], xn[4 * qd.value + 2], d0); d1 = mfma32(wf[3], xn[4 * qd.value + 3], d1);
        SCHED();
        if (qd.value == 0) { WQ_DMA(3); STAMP(13); }
      });
      st64(L + WUK + w * 4096 + q * 32 + g * 8, pk4(d0 + d1));
    }
    BAR();
    STAMP(4);
    {
      f4 d = {0.f, 0.f, 0.f, 0.f};
      v4u qf[4];
#pragma unroll
      for (int s = 0; s < 4; ++s) qf[s] = lds128(L + WUK + (2 * s + (g >> 1)) * 4096 + q * 32 + (g & 1) * 16);
      SCHED();
#pragma unroll
      for (int s = 0; s < 4; ++s) d = mfma32(kf[s], qf[s], d);
      constexpr float SC = 0.08838834764831845f * 1.4426950408889634f;
      st64(L + QAS + q * 256 + (16 * w + 4 * g) * 2, pk4(d * SC));
    }
    BAR();
    {
      const u32 set = ((h >> 2) << 6) | (j << 4) | ((h & 1) << 3) | (((h >> 1) & 1) << 2) | p;
      if (w == 0) {
        v4u v = lds128(L + QAS + q * 256 + (4 * g + p) * 16);
        v.w = (v.w & 0xFFFFu) | (tag16 << 16);
        STW128(v, WS_QA + (q * 5 + g) * 8192 + set * 16);
      } else if (w == 1 && lane < 16) {
        const u32 a = L + QAS + q * 256 + p * 16 + 14;
        const v4u v = {lds16(a) | (lds16(a + 64) << 16), lds16(a + 128) | (lds16(a + 192) << 16), 0u, tag16 << 16};
        STW128(v, WS_QA + (q * 5 + 4) * 8192 + set * 16);
      }
    }
    BAR();
    STAMP(5);
    UNTIL(KNOB(K_TWT));
    {
      const v4u s2 = mk_srd(wuv, 512u * 8192);
#pragma unroll
      for (int n = 0; n < 4; ++n) dma16(s2, n < 2 ? vo0 : vo1, so + 4 * n * 8192, L + WUV + (16 * w + 4 * n) * 256);
      const v4u s = mk_srd(wo, 4096u * 1024);
      const u32 vo = (lane >> 3) * 1024 + (((lane & 7) ^ (g & 1)) * 16);
#pragma unroll
      for (int n = 0; n < 16; ++n) {
        const u32 nn = (n + 4 * j) & 15;
        dma16(s, vo, (h * 128 + 8 * nn) * 1024 + w * 128, L + WO + w * 16384 + nn * 1024);
      }
    }
    VMCNT(16);
    BAR();
    v4u uv[4], of[4][4];
#pragma unroll
    for (int ks = 0; ks < 4; ++ks) uv[ks] = trfrag(L + WUV + tra + ks * 8192, L + WUV + tra + ks * 8192 + 1024);
    BAR();
    STAMP(6);
    VMCNT(0);
#pragma unroll
    for (int ct = 0; ct < 4; ++ct) {
      const u32 a = L + WO + w * 16384 + (8 * g + q) * 128 + (((2 * p + (ct >> 1)) ^ ((q >> 1) & 1)) * 16) + 8 * (ct & 1);
#pragma unroll
      for (int ks = 0; ks < 4; ++ks) of[ct][ks] = trfrag(a + ks * 4096, a + ks * 4096 + 512);
    }
    SCHED();
    STAMP(7);
    const u32 bb = w >> 1, sh = w & 1;
    {
      Regs<1> st;
      Regs<4> po;
      const u32 st_off = WS_ST + (bb * 32 + (lane & 31)) * 512 + h * 16;
      const u32 po_off = WS_PO + (u * 128 + w * 16) * 256 + lane * 16;
      if (USE_HINT) {
        if (w == 0) {
          for (int spin = 0; spin < SPIN_CAP; ++spin) {
            if (__builtin_amdgcn_raw_buffer_load_b32(rs, WS_HINT + 2048 + xcd * 256, 0, 16) == tag32) break;
            PAUSE();
          }
          SLEEP(KNOB(K_DPO));
        }
        BAR();
        STAMP(8);
        st.h = LDW(st_off);
        sfor<4>([&](auto n) { AT(po, n) = LDW(po_off + n.value * 1024); });
        gather<1, 0, true>(rs, st_off, v4u{0u, tag32, 0u, tag32}, v4u{0u, ~0u, 0u, ~0u}, st);
      } else {
        gather<1, 0>(rs, st_off, v4u{0u, tag32, 0u, tag32}, v4u{0u, ~0u, 0u, ~0u}, st);
        STAMP(8);
        SLEEP(KNOB(K_DPO));
        sfor<4>([&](auto n) { AT(po, n) = LDW(po_off + n.value * 1024); });
      }
      const float m = asf(st.h.x), M = max16x(rowmax16(m));
      const float e = __builtin_amdgcn_exp2f(m - M);
      const float wv = e * __builtin_amdgcn_rcpf(sum16x(rowsum16(e * asf(st.h.z))));
      float wgt[4];
#pragma unroll
      for (int n = 0; n < 4; ++n) wgt[n] = __shfl(wv, 16 * sh + 4 * n + g);
      SCHED();
      spins = gather<4, 1024, true>(rs, po_off, v4u{tb0, tb1, tb0, tb1}, v4u{0x10001u, 0x10001u, 0x10001u, 0x10001u}, po);
      COUNT(0, spins);
      STAMP(9);
      float acc[8];
#pragma unroll
      for (int e2 = 0; e2 < 8; ++e2) acc[e2] = 0.f;
      sfor<4>([&](auto n) {
        const v4u t = AT(po, n);
#pragma unroll
        for (int k = 0; k < 4; ++k) {
          acc[2 * k] += wgt[n.value] * asf((t[k] << 16) | 0x8000u);
          acc[2 * k + 1] += wgt[n.value] * asf((t[k] & 0xFFFF0000u) | 0x8000u);
        }
      });
      f4 r4;
#pragma unroll
      for (int k = 0; k < 4; ++k) {
        auto r = __builtin_amdgcn_permlane32_swap(asu(acc[k]), asu(acc[k + 4]), false, false);
        r4[k] = asf(r[0]) + asf(r[1]);
      }
      const u32 row = 4 * bb + 2 * sh + (g & 1);
      st64(L + OIMG + row * 256 + ((((i & 12) + 2 * (g >> 1) + (p >> 1)) ^ fsw(row)) * 16) + 8 * (p & 1), pk4(r4));
    }
    BAR();
    {
      f4 d = {0.f, 0.f, 0.f, 0.f};
      v4u bf[4];
#pragma unroll
      for (int ks = 0; ks < 4; ++ks) bf[ks] = lds128(L + OIMG + i * 256 + (((4 * ks + g) ^ fsw(i)) * 16));
      SCHED();
#pragma unroll
      for (int ks = 0; ks < 4; ++ks) d = mfma32(uv[ks], bf[ks], d);
      st64(L + CIMG + i * 256 + (((2 * w + (g >> 1)) ^ fsw(i)) * 16) + 8 * (g & 1), pk4(d));
    }
    BAR();
    {
      f4 d[4];
#pragma unroll
      for (int ct = 0; ct < 4; ++ct) d[ct] = f4{0.f, 0.f, 0.f, 0.f};
      v4u cf[4];
#pragma unroll
      for (int ks = 0; ks < 4; ++ks) cf[ks] = lds128(L + CIMG + i * 256 + (((4 * ks + g) ^ fsw(i)) * 16));
      SCHED();
#pragma unroll
      for (int ks = 0; ks < 4; ++ks)
#pragma unroll
        for (int ct = 0; ct < 4; ++ct) d[ct] = mfma32(of[ct][ks], cf[ks], d[ct]);
      f4 lo, hi, r;
#pragma unroll
      for (int e = 0; e < 4; ++e) {
        lo[e] = ((p & 1) ? d[1][e] : d[0][e]) + qp<0xB1>((p & 1) ? d[0][e] : d[1][e]);
        hi[e] = ((p & 1) ? d[3][e] : d[2][e]) + qp<0xB1>((p & 1) ? d[2][e] : d[3][e]);
        r[e] = ((p & 2) ? hi[e] : lo[e]) + qp<0x4E>((p & 2) ? lo[e] : hi[e]);
      }
      STW128(enc24v(r, tag8), WS_AP + u * 8192 + q * 2048 + (64 * w + 16 * g + 4 * p) * 4);
    }
    STAMP(10);
    if (USE_HINT && w == 1 && slot == 0 && lane == 0) __builtin_amdgcn_raw_buffer_store_b32(tag32, rs, WS_HINT + xcd * 256, 0, 16);
    if (w == 0) {
      const u32 col = (u >> 5) * 2048 + ((u & 31) * 16 + 4 * p) * 4;
      SLEEP(KNOB(K_DAP));
      f4 h1 = gather_reduce<8>(rs, WS_AP + (lane >> 2) * 8192 + col, tag8, spins);
      COUNT(1, spins);
      h1 += f4{bflo(xres.x), bfhi(xres.x), bflo(xres.y), bfhi(xres.y)};
      if (lane < 32) STW128(enc24v(h1, tag8), WS_H1 + (lane >> 2) * 8192 + col);
      STAMP(11);
      if (FP_HINT) {
        Regs<8> t;
        gather<8, 1024>(rs, WS_H1 + xcd * 8192 + lane * 16, key8, msk8, t);
        STAMP(3);
      }
      SLEEP(KNOB(K_DFP));
      const f4 y = h1 + gather_reduce<7>(rs, WS_FP + (lane >> 2) * 8192 + col, tag8, spins);
      COUNT(2, spins);
      if (lane < 4) *(v2u*)(out + (col >> 2)) = pk4(y);
      if (u == 0 && lane == 0) __hip_atomic_store((u32*)ws, tag32, __ATOMIC_RELAXED, __HIP_MEMORY_SCOPE_AGENT);
      STAMP(12);
    }
    STAMP_FLUSH();
  }
}

extern "C" void kernel_launch(void* const* d_in, const int* in_sizes, int n_in, void* d_out, int out_size, void* d_ws, size_t ws_size,
                              hipStream_t stream) {
  (void)hipFuncSetAttribute((const void*)fused_decode, hipFuncAttributeMaxDynamicSharedMemorySize, (int)LDS_BYTES);
  hipLaunchKernelGGL(fused_decode, dim3(256), dim3(512), LDS_BYTES, stream, (const u16*)d_in[0], (const u16*)d_in[1], (const u16*)d_in[2],
                     (const u16*)d_in[3], (const u16*)d_in[4], (const u16*)d_in[5], (const u16*)d_in[7], (const u16*)d_in[8],
                     (const u16*)d_in[9], (u16*)d_out, (unsigned char*)d_ws);
}
```

```cpp
#include <hip/hip_runtime.h>
#include <stdint.h>
#include <utility>

typedef unsigned u32;
typedef unsigned short u16;
typedef u32 v4u __attribute__((ext_vector_type(4)));
typedef u32 v2u __attribute__((ext_vector_type(2)));
typedef float f4 __attribute__((ext_vector_type(4)));
typedef float f2 __attribute__((ext_vector_type(2)));
typedef float f16v __attribute__((ext_vector_type(16)));
typedef short s8 __attribute__((ext_vector_type(8)));
typedef short s4 __attribute__((ext_vector_type(4)));
typedef __bf16 bf2 __attribute__((ext_vector_type(2)));
#define LAS __attribute__((address_space(3)))
#define DI __device__ __forceinline__

constexpr u32 WS_QA = 0;
constexpr u32 WS_ST = WS_QA + 131072;
constexpr u32 WS_H1 = WS_ST + 65536;
constexpr u32 WS_HINT = WS_H1 + 65536;
constexpr u32 WS_AP = WS_HINT + 4096;
constexpr u32 WS_FP = WS_AP + 1048576;
constexpr u32 WS_PO = WS_FP + 128 * 8192;
constexpr u32 WS_END = WS_PO + 32 * 4 * 128 * 256;
constexpr u32 WS_BASE = 4096;
constexpr int SPIN_CAP = 1 << 17;
constexpr u32 LDS_BYTES = 163840;
constexpr int CODE_PF = 13;
constexpr int S_KV = 20, T_QA = 455, T_WT = 450, S_PO = 5, D_AP = 226, D_H1 = 108, S_H1 = 26, S_FP = 42, GU_EARLY = 3;

DI v4u mk_srd(const void* p, u32 bytes) {
  unsigned long long a = (unsigned long long)p;
  return v4u{(u32)a, (u32)(a >> 32) & 0xFFFFu, bytes, 0x00020000u};
}
template <bool NT> DI void dma16(v4u srd, u32 voff, u32 soff, u32 lds) {
  u32 keep;
  if constexpr (NT)
    asm volatile("s_mov_b32 %0, m0\n\ts_mov_b32 m0, %4\n\ts_nop 2\n\tbuffer_load_dwordx4 %1, %2, %3 offen nt lds\n\ts_mov_b32 m0, %0"
                 : "=&s"(keep) : "v"(voff), "s"(srd), "s"(soff), "s"(lds) : "memory");
  else
    asm volatile("s_mov_b32 %0, m0\n\ts_mov_b32 m0, %4\n\ts_nop 2\n\tbuffer_load_dwordx4 %1, %2, %3 offen lds\n\ts_mov_b32 m0, %0"
                 : "=&s"(keep) : "v"(voff), "s"(srd), "s"(soff), "s"(lds) : "memory");
}
DI void dma4(v4u srd, u32 voff, u32 soff, u32 lds) {
  u32 keep;
  asm volatile("s_mov_b32 %0, m0\n\ts_mov_b32 m0, %4\n\ts_nop 2\n\tbuffer_load_dword %1, %2, %3 offen lds\n\ts_mov_b32 m0, %0"
               : "=&s"(keep) : "v"(voff), "s"(srd), "s"(soff), "s"(lds) : "memory");
}
template <int N> DI void vmcnt() { asm volatile("s_waitcnt vmcnt(%0)" ::"i"(N) : "memory"); }
#define VMCNT(n) asm volatile("s_waitcnt vmcnt(" #n ")" ::: "memory")
#define BAR() asm volatile("s_waitcnt lgkmcnt(0)\n\ts_barrier" ::: "memory")
#define CFENCE() asm volatile("" ::: "memory")
#define SCHED() __builtin_amdgcn_sched_barrier(0)
#define NOW() ((u32)__builtin_amdgcn_s_memrealtime() - t_start)
#define UNTIL(t) do { const u32 t_ = (t); while (NOW() < t_) __builtin_amdgcn_s_sleep(1); } while (0)
#define SLEEP(n) do { const int n_ = (n); _Pragma("nounroll") for (int z_ = 0; z_ < n_; ++z_) __builtin_amdgcn_s_sleep(1); } while (0)

DI v4u lds128(u32 a) { return *(LAS v4u*)(uintptr_t)a; }
DI f4 lds128f(u32 a) { return *(LAS f4*)(uintptr_t)a; }
DI v2u lds64(u32 a) { return *(LAS v2u*)(uintptr_t)a; }
DI float lds32f(u32 a) { return *(LAS float*)(uintptr_t)a; }
DI void st128(u32 a, v4u v) { *(LAS v4u*)(uintptr_t)a = v; }
DI void st64(u32 a, v2u v) { *(LAS v2u*)(uintptr_t)a = v; }
DI void st32f(u32 a, float v) { *(LAS float*)(uintptr_t)a = v; }
DI void st16(u32 a, u16 v) { *(LAS u16*)(uintptr_t)a = v; }
DI v2u tr64(u32 a) { return __builtin_bit_cast(v2u, __builtin_amdgcn_ds_read_tr16_b64_v4i16((LAS s4*)(uintptr_t)a)); }
DI v4u trfrag(u32 a0, u32 a1) { v2u x = tr64(a0), y = tr64(a1); return v4u{x.x, x.y, y.x, y.y}; }

DI float asf(u32 v) { return __builtin_bit_cast(float, v); }
DI u32 asu(float v) { return __builtin_bit_cast(u32, v); }
DI float bflo(u32 v) { return asf(v << 16); }
DI float bfhi(u32 v) { return asf(v & 0xFFFF0000u); }
DI u32 pkbf(float a, float b) { f2 v = {a, b}; return __builtin_bit_cast(u32, __builtin_convertvector(v, bf2)); }
DI v2u pk4(f4 a) { return v2u{pkbf(a[0], a[1]), pkbf(a[2], a[3])}; }
DI f4 mfma32(v4u a, v4u b, f4 c) { return __builtin_amdgcn_mfma_f32_16x16x32_bf16(__builtin_bit_cast(s8, a), __builtin_bit_cast(s8, b), c, 0, 0, 0); }
DI f4 mfma16(v2u a, v2u b, f4 c) { return __builtin_amdgcn_mfma_f32_16x16x16bf16_1k(__builtin_bit_cast(s4, a), __builtin_bit_cast(s4, b), c, 0, 0, 0); }
DI f16v mfmaL(v4u a, v4u b, f16v c) { return __builtin_amdgcn_mfma_f32_32x32x16_bf16(__builtin_bit_cast(s8, a), __builtin_bit_cast(s8, b), c, 0, 0, 0); }
template <int N> DI float ror(float v) { return asf((u32)__builtin_amdgcn_update_dpp(0, (int)asu(v), 0x120 + N, 0xf, 0xf, false)); }
template <int C> DI float qp(float v) { return asf((u32)__builtin_amdgcn_update_dpp(0, (int)asu(v), C, 0xf, 0xf, false)); }
DI float mx(float a, float b) { return __builtin_elementwise_maximum(a, b); }
DI float mx4(f4 a) { return mx(mx(a[0], a[1]), mx(a[2], a[3])); }
DI float sum4(f4 a) { return (a[0] + a[1]) + (a[2] + a[3]); }
DI float sum16x(float v) { auto r = __builtin_amdgcn_permlane16_swap(asu(v), asu(v), false, false); return asf(r[0]) + asf(r[1]); }
DI float sum32x(float v) { auto r = __builtin_amdgcn_permlane32_swap(asu(v), asu(v), false, false); return asf(r[0]) + asf(r[1]); }
DI float max16x(float v) { auto r = __builtin_amdgcn_permlane16_swap(asu(v), asu(v), false, false); return mx(asf(r[0]), asf(r[1])); }
DI float max32x(float v) { auto r = __builtin_amdgcn_permlane32_swap(asu(v), asu(v), false, false); return mx(asf(r[0]), asf(r[1])); }
DI float rowsum16(float v) { v += ror<8>(v); v += ror<4>(v); v += ror<2>(v); v += ror<1>(v); return v; }
DI float rowmax16(float v) { v = mx(v, ror<8>(v)); v = mx(v, ror<4>(v)); v = mx(v, ror<2>(v)); v = mx(v, ror<1>(v)); return v; }
DI u32 fsw(u32 row) { return ((row & 3u) | (((row >> 3) & 1u) << 2)) << 1; }
DI u32 fsw5(u32 row) { return fsw(row) | ((row >> 4) & 1u); }
DI f4 sel4(bool c, f4 a, f4 b) { return f4{c ? a[0] : b[0], c ? a[1] : b[1], c ? a[2] : b[2], c ? a[3] : b[3]}; }
DI u32 bfi(u32 m, u32 a, u32 b) { return (m & a) | (~m & b); }
DI u32 enc24(float v, u32 t) { return ((asu(v) + 0x80u) & 0xFFFFFF00u) | t; }
DI v4u enc24v(f4 r, u32 t) { return v4u{enc24(r[0], t), enc24(r[1], t), enc24(r[2], t), enc24(r[3], t)}; }

#define LDW2(off, so) __builtin_amdgcn_raw_buffer_load_b128(rs, (int)(off), (int)(so), 16)
#define STW2(v, off, so) __builtin_amdgcn_raw_buffer_store_b128(v, rs, (int)(off), (int)(so), 16)
#define PIN(x) asm volatile("" : "+v"(x))
#define STW128(v, off) __builtin_amdgcn_raw_buffer_store_b128(v, rs, (int)(off), 0, 16)
#define PAUSE() do { __builtin_amdgcn_s_sleep(1); CFENCE(); } while (0)

template <int... Is, class F> DI void sfor_(std::integer_sequence<int, Is...>, F f) { (f(std::integral_constant<int, Is>{}), ...); }
template <int N, class F> DI void sfor(F f) { sfor_(std::make_integer_sequence<int, N>{}, f); }
template <int N> struct Regs { v4u h; Regs<N - 1> t; template <int I> DI v4u& at() { if constexpr (I == 0) return h; else return t.template at<I - 1>(); } };
template <> struct Regs<1> { v4u h; template <int I> DI v4u& at() { return h; } };
#define AT(a, n) (a).template at<(n).value>()
template <int NL, int STRIDE, bool ISSUED = false, int BASE = 0> DI void gather(__amdgpu_buffer_rsrc_t rs, u32 off, v4u key, v4u mask, Regs<NL>& a) {
  if (!ISSUED) sfor<NL>([&](auto n) { AT(a, n) = LDW2(off, BASE + n.value * STRIDE); });
  u32 pend = 0;
  sfor<NL>([&](auto n) {
    AT(a, n) ^= key;
    const v4u t = AT(a, n) & mask;
    if (!__all((t.x | t.y | t.z | t.w) == 0u)) pend |= 1u << n.value;
  });
  int spin = 0;
  while (pend != 0u && spin < SPIN_CAP) {
    ++spin;
    PAUSE();
    sfor<NL>([&](auto n) { if (pend >> n.value & 1u) AT(a, n) = LDW2(off, BASE + n.value * STRIDE); });
    sfor<NL>([&](auto n) {
      if (pend >> n.value & 1u) {
        AT(a, n) ^= key;
        const v4u t = AT(a, n) & mask;
        if (__all((t.x | t.y | t.z | t.w) == 0u)) pend &= ~(1u << n.value);
      }
    });
  }
}
DI f4 lane_reduce(f4 s) {
  auto r0 = __builtin_amdgcn_permlane32_swap(asu(s[0]), asu(s[2]), false, false);
  auto r1 = __builtin_amdgcn_permlane32_swap(asu(s[1]), asu(s[3]), false, false);
  float a0 = asf(r0[0]) + asf(r0[1]), a1 = asf(r1[0]) + asf(r1[1]);
  auto r2 = __builtin_amdgcn_permlane16_swap(asu(a0), asu(a1), false, false);
  float c = asf(r2[0]) + asf(r2[1]);
  c += ror<8>(c); c += ror<4>(c);
  auto r3 = __builtin_amdgcn_permlane16_swap(asu(c), asu(c), false, false);
  auto r4 = __builtin_amdgcn_permlane32_swap(r3[0], r3[0], false, false);
  auto r5 = __builtin_amdgcn_permlane32_swap(r3[1], r3[1], false, false);
  return f4{asf(r4[0]), asf(r5[0]), asf(r4[1]), asf(r5[1])};
}

__global__ void __launch_bounds__(512, 2)
fused_decode(const u16* __restrict__ x, const u16* __restrict__ ckv, const u16* __restrict__ wq, const u16* __restrict__ wuk,
             const u16* __restrict__ wuv, const u16* __restrict__ wo, const u16* __restrict__ wg, const u16* __restrict__ wu,
             const u16* __restrict__ wd, u16* __restrict__ out, unsigned char* ws) {
  extern __shared__ __attribute__((aligned(16))) unsigned char smem[];
  const u32 L = (u32)(uintptr_t)(LAS unsigned char*)smem;
  if ((blockIdx.x & 8) == 0 && __builtin_amdgcn_readfirstlane(threadIdx.x) < 64) {
    unsigned long long pc;
    asm volatile("s_getpc_b64 %0" : "=s"(pc));
    dma4(mk_srd((const void*)((pc & ~127ull) + (blockIdx.x >> 4) * (CODE_PF * 128)), CODE_PF * 128), threadIdx.x * 128, 0, L + 131072);
  }
  asm volatile("" ::"s"(x), "s"(ckv), "s"(wq), "s"(wuk), "s"(wuv), "s"(wo), "s"(wg), "s"(wu), "s"(wd), "s"(out), "s"(ws) : "memory");
  const u32 tid = threadIdx.x, lane = tid & 63;
  const u32 w = __builtin_amdgcn_readfirstlane(tid >> 6);
  const u32 bid = blockIdx.x, xcd = bid & 7, role = (bid >> 3) & 1, slot = bid >> 4, u = xcd * 16 + slot;
  const u32 i = lane & 15, g = lane >> 4, q = i >> 2, p = lane & 3;
  const u32 t_start = (u32)__builtin_amdgcn_s_memrealtime();
  u32 Ev = *(const __attribute__((address_space(4))) u32*)(uintptr_t)ws;
#define EPOCH_SETUP()                                                                                                              \
  asm volatile("" : "+s"(Ev));                                                                \
  const u32 tag32 = Ev + 1u, tag8 = tag32 & 0xFFu;                                                         \
  const __amdgpu_buffer_rsrc_t rs = __builtin_amdgcn_make_buffer_rsrc((void*)(ws + WS_BASE), 0, (int)WS_END, 0x00020000); \
  const u32 tb0 = (tag32 & 1u) | ((tag32 & 2u) << 15), tb1 = ((tag32 >> 2) & 1u) | ((tag32 & 8u) << 13);                           \
  const v4u key8 = {tag8, tag8, tag8, tag8}, msk8 = {0xFFu, 0xFFu, 0xFFu, 0xFFu};

  if (role == 0) {
    constexpr u32 QIMG = 131072, DN = 32768, PIMG = 65536, MX = 73728, LS = 74752, XN = 77824, RED = 90112, ACT = 94208, STAT = 94464, TR = 98304;
    const u32 b = u >> 5, h32 = lane & 31, hf = lane >> 5;
    const bool ffn = slot < 14;
    const u32 fu = xcd * 14 + slot;
    const u32 GU = w * 4096;
    const v4u sgu = mk_srd((w & 1) ? wu : wg, 512u * 1792 * 2);
    const u32 gu_vo = ((lane >> 1) ^ ((((lane >> 1) >> 3) & 1) << 2)) * 3584 + (lane & 1) * 16;
#define GU_DMA(n) dma16<true>(sgu, gu_vo, ((w >> 1) * 128 + 32 * (n)) * 3584 + fu * 32, L + GU + (n) * 1024)
    EPOCH_SETUP();
    SLEEP(S_KV);
    const v4u skv = mk_srd(ckv, 4u * 4096 * 1024);
    const u32 kvx = (((w >> 1) & 1) << 3) | (w >> 2);
#define KV_ROW(k) (b * 4096 + (u & 31) * 4 + ((k) & 3) + 128 * ((k) >> 2))
#define KV_DMA(st) _Pragma("unroll") for (int r = 0; r < 4; ++r) dma16<true>(skv, (lane ^ kvx ^ (r << 1)) * 16, KV_ROW(32 * (st) + 4 * w + r) * 1024, L + (32 * (st) + 4 * w + r) * 1024)
    v4u fr[32];
    Regs<4> qa;
    const u32 qa_off = WS_QA + b * 4 * 8192 + tid * 16;
    bool got = false;
#define QA_TRY() do { if (!got && NOW() >= (u32)T_QA) { got = true; sfor<4>([&](auto n) { AT(qa, n) = LDW2(qa_off, n.value * 8192); }); } } while (0)
#define KV_FRAGS(st)                                                                                                            \
  do {                                                                                                                          \
    if (w == (st)) {                                                                                                            \
      const u32 kb = L + (32 * (st) + h32) * 1024;                                                                              \
      _Pragma("unroll") for (int s = 0; s < 32; ++s) fr[s] = lds128(kb + (((2 * (s & 7) + hf) ^ fsw5(h32)) * 16) + (s >> 3) * 256); \
    } else if (w >= 4) {                                                                                                        \
      const u32 fv = ((q | ((g & 1) << 2)) << 1) | (g >> 1);                                                                    \
      _Pragma("unroll") for (int ct = 0; ct < 8; ++ct) {                                                                        \
        const u32 vb = L + (32 * (st) + 8 * g + q) * 1024 + (w - 4) * 256 + (((2 * ct + (p >> 1)) ^ fv) * 16) + 8 * (p & 1);    \
        fr[4 * ct + (st)] = trfrag(vb, vb + 4096);                                                                              \
      }                                                                                                                         \
    }                                                                                                                           \
    SCHED();                                                                                                                    \
  } while (0)
    KV_DMA(0);
    KV_DMA(1);
    VMCNT(4);
    BAR();
    KV_DMA(2);
    KV_FRAGS(0);
    VMCNT(4);
    BAR();
    KV_DMA(3);
    QA_TRY();
    KV_FRAGS(1);
    QA_TRY();
    if (got) VMCNT(8); else VMCNT(4);
    BAR();
    QA_TRY();
    KV_FRAGS(2);
    if (!got) {
      UNTIL(T_QA);
      sfor<4>([&](auto n) { AT(qa, n) = LDW2(qa_off, n.value * 8192); });
    }
    VMCNT(4);
    BAR();
    KV_FRAGS(3);
    u32 ad[8], aw[4], pa[4], mxa = L + MX + h32 * 16;
    const u32 hh = (w << 2) | (((lane >> 2) & 1) << 1) | ((lane >> 3) & 1);
    u32 qw[4];
#pragma unroll
    for (int n = 0; n < 4; ++n) { qw[n] = L + QIMG + hh * 1024 + (((16 * g + 4 * n + p) ^ fsw5(hh)) * 16); PIN(qw[n]); }
    if (w < 4) {
#pragma unroll
      for (int s = 0; s < 8; ++s) ad[s] = L + QIMG + h32 * 1024 + (((2 * s + hf) ^ fsw5(h32)) * 16);
#pragma unroll
      for (int n = 0; n < 4; ++n) { pa[n] = L + PIMG + h32 * 256 + (((4 * w + n) ^ fsw(h32)) * 16) + 8 * hf; aw[n] = 0; }
      aw[0] = WS_ST + u * 512 + lane * 16;
    } else {
      const u32 T = L + TR + (w - 4) * 8192;
#pragma unroll
      for (int n = 0; n < 4; ++n) pa[n] = L + PIMG + i * 256 + (((4 * n + g) ^ fsw(i)) * 16);
#pragma unroll
      for (int n = 0; n < 4; ++n) aw[n] = T + i * 256 + (((4 * n + g) ^ (i & 7)) * 16);
      ad[0] = tb0; ad[1] = tb1;
      ad[2] = T + g * 256 + ((i ^ g) * 16);
      ad[3] = T + (4 + g) * 256 + ((i ^ (4 + g)) * 16);
      ad[4] = WS_PO + ((g * 4 + (w - 4)) * 128 + u) * 256 + i * 16;
      ad[5] = ad[6] = ad[7] = 0;
    }
#pragma unroll
    for (int s = 0; s < 8; ++s) PIN(ad[s]);
#pragma unroll
    for (int n = 0; n < 4; ++n) { PIN(pa[n]); PIN(aw[n]); }
    PIN(mxa);
    SCHED();
    gather<4, 8192, true>(rs, qa_off, v4u{tb0, tb1, tb0, tb1}, v4u{0x10001u, 0x10001u, 0x10001u, 0x10001u}, qa);
    {
      sfor<4>([&](auto n) { st128(qw[n.value], AT(qa, n)); });
    }
    BAR();
    f16v sc;
    if (w < 4) {
#pragma unroll
      for (int e = 0; e < 16; ++e) sc[e] = 0.f;
      v4u qf[8];
#pragma unroll
      for (int s = 0; s < 8; ++s) qf[s] = lds128(ad[s]);
      SCHED();
#pragma unroll
      for (int s = 0; s < 32; ++s) {
        sc = mfmaL(fr[s], qf[s & 7], sc);
        if (s < 24) qf[s & 7] = lds128(ad[s & 7] + ((s >> 3) + 1) * 256);
        SCHED();
      }
      float m = mx(mx(mx(sc[0], sc[1]), mx(sc[2], sc[3])), mx(mx(sc[4], sc[5]), mx(sc[6], sc[7])));
      m = mx(m, mx(mx(mx(sc[8], sc[9]), mx(sc[10], sc[11])), mx(mx(sc[12], sc[13]), mx(sc[14], sc[15]))));
      st32f(mxa + w * 4, max32x(m));
    } else if (ffn) {
#pragma unroll
      for (int n = 0; n < 4; ++n) if (n < GU_EARLY) GU_DMA(n);
    }
    BAR();
    if (w < 4) {
      const float m = mx4(lds128f(mxa));
      f4 l = {0.f, 0.f, 0.f, 0.f};
#pragma unroll
      for (int rg = 0; rg < 4; ++rg) {
        f4 e;
#pragma unroll
        for (int r = 0; r < 4; ++r) e[r] = __builtin_amdgcn_exp2f(sc[4 * rg + r] - m);
        st64(pa[rg], pk4(e));
        l += e;
      }
      st32f(mxa + (LS - MX) + w * 4, sum32x(sum4(l)));
    }
    BAR();
    if (w == 0 && lane < 32) STW128((v4u{asu(mx4(lds128f(mxa))), tag32, asu(sum4(lds128f(mxa + (LS - MX)))), tag32}), aw[0]);
    if (w >= 4) {
      v4u pf[2][4];
#pragma unroll
      for (int ks = 0; ks < 4; ++ks) pf[0][ks] = lds128(pa[ks]);
#pragma unroll
      for (int ks = 0; ks < 4; ++ks) pf[1][ks] = lds128(pa[ks] + 4096);
      SCHED();
      u32 mk = 0xFFFEFFFEu;
      asm volatile("" : "+s"(mk));
      f4 o[2][8];
      v4u t[4];
#define PO_DW(ht, k, e0, tv) bfi(mk, pkbf(o[ht][k][e0], o[ht][k][e0 + 1]), tv)
#define PO_PACK(ht, tp) st128(aw[tp] + (ht) * 4096, v4u{PO_DW(ht, 2 * (tp), 0, ad[0]), PO_DW(ht, 2 * (tp), 2, ad[1]), PO_DW(ht, 2 * (tp) + 1, 0, ad[0]), PO_DW(ht, 2 * (tp) + 1, 2, ad[1])})
#define PO_READ(ht) _Pragma("unroll") for (int n = 0; n < 4; ++n) t[n] = lds128(ad[2 + (n & 1)] + (ht) * 4096 + (n >> 1) * 2048)
#define PO_STORE(ht) _Pragma("unroll") for (int n = 0; n < 4; ++n) STW2(t[n], ad[4], (4 * (ht) + n) * (4 * 4 * 128 * 256))
#pragma unroll
      for (int ks = 0; ks < 4; ++ks)
#pragma unroll
        for (int ct = 0; ct < 8; ++ct) o[0][ct] = mfma32(fr[4 * ct + ks], pf[0][ks], ks == 0 ? f4{0.f, 0.f, 0.f, 0.f} : o[0][ct]);
      SCHED();
#pragma unroll
      for (int ks = 0; ks < 4; ++ks) {
#pragma unroll
        for (int ct = 0; ct < 8; ++ct) o[1][ct] = mfma32(fr[4 * ct + ks], pf[1][ks], ks == 0 ? f4{0.f, 0.f, 0.f, 0.f} : o[1][ct]);
        if (ks == 0) { PO_PACK(0, 0); PO_PACK(0, 1); }
        if (ks == 1) { PO_PACK(0, 2); PO_PACK(0, 3); }
        if (ks == 2) { PO_READ(0); }
        if (ks == 3) { PO_STORE(0); }
        if (ks < 2) {
#pragma unroll
          for (int k = 0; k < 8; ++k) { __builtin_amdgcn_sched_group_barrier(0x8, 1, 0); __builtin_amdgcn_sched_group_barrier(0x2, 2, 0); }
        }
        SCHED();
      }
      PO_PACK(1, 0); PO_PACK(1, 1); PO_PACK(1, 2); PO_PACK(1, 3);
      PO_READ(1);
      PO_STORE(1);
    }
    if (ffn) {
      BAR();
#pragma unroll
      for (int n = 0; n < 4; ++n) if (w < 4 || n >= GU_EARLY) GU_DMA(n);
      {
        const v4u sd = mk_srd(wd, 1792u * 512 * 2);
        const u32 r8 = lane >> 3;
#pragma unroll
        for (int n = 0; n < 2; ++n) dma16<true>(sd, r8 * 1024 + (((lane & 7) ^ ((r8 >> 1) & 1)) * 16), (fu * 16 + 8 * n) * 1024 + w * 128, L + DN + w * 2048 + n * 1024);
      }
      VMCNT(0);
      v4u gf[4];
      v2u df[4];
      {
        const u32 r0 = 8 * g + q, sw = (g & 1) << 2;
#pragma unroll
        for (int ks = 0; ks < 4; ++ks) {
          const u32 a = L + GU + ks * 1024 + p * 8;
          gf[ks] = trfrag(a + ((r0) ^ sw) * 32, a + ((r0 + 4) ^ sw) * 32);
        }
        const u32 rd = 4 * g + q;
#pragma unroll
        for (int ct = 0; ct < 4; ++ct) df[ct] = tr64(L + DN + w * 2048 + rd * 128 + (((2 * p + (ct >> 1)) ^ ((rd >> 1) & 1)) * 16) + 8 * (ct & 1));
      }
      float g1 = 0.f, u1 = 0.f;
      {
        f4 d = {0.f, 0.f, 0.f, 0.f};
#pragma unroll
        for (int ks = 0; ks < 4; ++ks) d = mfma32(gf[ks], v4u{0x3F803F80u, 0x3F803F80u, 0x3F803F80u, 0x3F803F80u}, d);
        st128(L + RED + 2048 + w * 64 + g * 16, __builtin_bit_cast(v4u, d));
        BAR();
        const u32 a = L + RED + 2048 + i * 4;
        g1 = (lds32f(a) + lds32f(a + 128)) + (lds32f(a + 256) + lds32f(a + 384));
        u1 = (lds32f(a + 64) + lds32f(a + 192)) + (lds32f(a + 320) + lds32f(a + 448));
      }
      v4u xf[4];
      u32 xw = L + XN + (w & 3) * 1056 + lane * 8, xa = L + XN + (q + 4 * (p & 1)) * 1056 + ((w >> 1) * 128 + 8 * g) * 2;
      u32 rw = L + RED + w * 256 + q * 64 + g * 16, ra = L + RED + lane * 4, aa = L + ACT + q * 32 + g * 8;
      u32 fo = WS_FP + (q * 32 + 4 * w + g) * 8192 + fu * 64 + p * 16, ho = WS_H1 + xcd * 8192 + (w & 3) * 2048 + lane * 16;
      PIN(xw); PIN(xa); PIN(rw); PIN(ra); PIN(aa); PIN(fo); PIN(ho); PIN(g1); PIN(u1);
      SCHED();
      v2u hint = {0u, 0u};
      for (int spin = 0; spin < SPIN_CAP; ++spin) {
        hint = __builtin_amdgcn_raw_buffer_load_b64(rs, WS_HINT + xcd * 256, 0, 16);
        if (hint.y == tag32) break;
        PAUSE();
      }
      UNTIL(min((u32)__builtin_amdgcn_readfirstlane(hint.x) - t_start, NOW()) + D_H1);
      Regs<2> hh;
      gather<2, 1024>(rs, ho, key8, msk8, hh);
      f4 fa = __builtin_bit_cast(f4, hh.at<0>()), fc = __builtin_bit_cast(f4, hh.at<1>());
      float mean = 0.f;
      if (w < 4) {
        const v2u ha = pk4(fa), hc = pk4(fc);
        st64(xw, ha);
        st64(xw + 512, hc);
        st64(xw + 4 * 1056, pk4(fa - f4{bflo(ha.x), bfhi(ha.x), bflo(ha.y), bfhi(ha.y)}));
        st64(xw + 4 * 1056 + 512, pk4(fc - f4{bflo(hc.x), bfhi(hc.x), bflo(hc.y), bfhi(hc.y)}));
      } else {
        mean = sum32x(sum16x(rowsum16(sum4(fa + fc)))) * (1.0f / 512.0f);
      }
      BAR();
#pragma unroll
      for (int ks = 0; ks < 4; ++ks) xf[ks] = lds128(xa + 64 * ks);
      SCHED();
      float v = 0.f;
      if (w >= 4) {
        fa -= mean; fc -= mean;
        v = rowsum16(sum4(fa * fa + fc * fc));
      }
      SCHED();
      f4 d = {0.f, 0.f, 0.f, 0.f};
#pragma unroll
      for (int ks = 0; ks < 4; ++ks) d = mfma32(gf[ks], xf[ks], d);
      SCHED();
      if (w >= 4) st64(L + STAT + (w - 4) * 8, v2u{asu(mean), asu(__builtin_amdgcn_rsqf(sum32x(sum16x(v)) * (1.0f / 512.0f) + 1e-5f))});
#pragma unroll
      for (int e = 0; e < 4; ++e) d[e] += qp<0xB1>(d[e]);
      st128(rw, __builtin_bit_cast(v4u, d));
      BAR();
      if (w == 0) {
        const v2u ms = lds64(L + STAT + g * 8);
        const float g0 = (lds32f(ra) + lds32f(ra + 512)) + (lds32f(ra + 1024) + lds32f(ra + 1536));
        const float u0 = (lds32f(ra + 256) + lds32f(ra + 768)) + (lds32f(ra + 1280) + lds32f(ra + 1792));
        const float gg = (g0 - asf(ms.x) * g1) * asf(ms.y), uu = (u0 - asf(ms.x) * u1) * asf(ms.y);
        const float act = gg * __builtin_amdgcn_rcpf(1.0f + __builtin_amdgcn_exp2f(gg * -1.4426950408889634f)) * uu;
        st16(L + ACT + lane * 2, (u16)(pkbf(act, act) & 0xFFFFu));
      }
      BAR();
      const v2u af = lds64(aa);
      f4 dd[4];
#pragma unroll
      for (int ct = 0; ct < 4; ++ct) dd[ct] = mfma16(df[ct], af, f4{0.f, 0.f, 0.f, 0.f});
      const f4 r = sel4(p & 2, sel4(p & 1, dd[3], dd[2]), sel4(p & 1, dd[1], dd[0]));
      STW128(enc24v(r, tag8), fo);
    }
  } else {
    constexpr u32 WQ = 0, WUK = 131072, X = WUK + 28672, QAS = 0, WUV = 0, WO = 32768, OIMG = 0, CIMG = 4096, WGT = 8192;
    const u32 h = u >> 2, j = u & 3;
    const u32 vo0 = g * 8192 + ((i ^ (g << 1)) * 16), vo1 = g * 8192 + ((i ^ ((g | 4) << 1)) * 16);
    const u32 so = (128 * j + 16 * w) * 8192 + h * 256;
    v4u kf[4];
    if (w == 0) {
#pragma unroll
      for (int n = 0; n < 4; ++n) dma16<false>(mk_srd(x, 4096), lane * 16, n * 1024, L + X + n * 1024);
    }
    if (w < 7) {
      const v4u s2 = mk_srd(wuk, 512u * 8192);
#pragma unroll
      for (int n = 0; n < 4; ++n) dma16<false>(s2, n < 2 ? vo0 : vo1, so + 4 * n * 8192, L + WUK + (16 * w + 4 * n) * 256);
    } else {
#pragma unroll
      for (int s = 0; s < 4; ++s) kf[s] = __builtin_nontemporal_load((const v4u*)(wuk + (size_t)(128 * j + 112 + i) * 4096 + h * 128 + 32 * s + 8 * g));
    }
    const v4u swq = mk_srd(wq, 512u * 8192);
#define WQ_DMA(qd)                                                                                                              \
  do {                                                                                                                          \
    const u32 r0_ = 128 * (((qd) + j) & 3) + 16 * w;                                                                            \
    _Pragma("unroll") for (int n = 0; n < 4; ++n) dma16<false>(swq, n < 2 ? vo0 : vo1, (r0_ + 4 * n) * 8192 + h * 256, L + WQ + (r0_ + 4 * n) * 256); \
  } while (0)
    WQ_DMA(0);
    asm volatile("s_barrier" ::: "memory");
    v2u xres = {0u, 0u};
    if (w == 0) {
      VMCNT(8);
      xres = lds64(L + X + (u >> 5) * 1024 + ((u & 31) * 16 + 4 * p) * 2);
      f4 v[8];
#pragma unroll
      for (int n = 0; n < 4; ++n) {
        const v4u t = lds128(L + X + q * 1024 + (4 * g + p) * 64 + n * 16);
        v[2 * n] = f4{bflo(t.x), bfhi(t.x), bflo(t.y), bfhi(t.y)};
        v[2 * n + 1] = f4{bflo(t.z), bfhi(t.z), bflo(t.w), bfhi(t.w)};
      }
      float s = sum4(((v[0] + v[1]) + (v[2] + v[3])) + ((v[4] + v[5]) + (v[6] + v[7])));
      s += qp<0xB1>(s); s += qp<0x4E>(s); s = sum32x(sum16x(s));
      const float mean = s * (1.0f / 512.0f);
#pragma unroll
      for (int n = 0; n < 8; ++n) v[n] -= mean;
      float vv = sum4(((v[0] * v[0] + v[1] * v[1]) + (v[2] * v[2] + v[3] * v[3])) + ((v[4] * v[4] + v[5] * v[5]) + (v[6] * v[6] + v[7] * v[7])));
      vv += qp<0xB1>(vv); vv += qp<0x4E>(vv); vv = sum32x(sum16x(vv));
      const float r = __builtin_amdgcn_rsqf(vv * (1.0f / 512.0f) + 1e-5f);
#pragma unroll
      for (int n = 0; n < 4; ++n) {
        const v2u a = pk4(v[2 * n] * r), c = pk4(v[2 * n + 1] * r);
        st128(L + X + q * 1024 + (4 * g + p) * 64 + n * 16, v4u{a.x, a.y, c.x, c.y});
      }
    }
    WQ_DMA(1);
    BAR();
    EPOCH_SETUP();
    const u32 tra = (8 * g + q) * 256 + (((2 * w + (p >> 1)) ^ ((q | ((g & 1) << 2)) << 1)) * 16) + 8 * (p & 1);
    {
      v4u xn[16];
#pragma unroll
      for (int ks = 0; ks < 16; ++ks) xn[ks] = lds128(L + X + q * 1024 + ((ks * 64 + j * 256) & 1023) + g * 16);
      SCHED();
      WQ_DMA(2);
      f4 d0 = {0.f, 0.f, 0.f, 0.f}, d1 = {0.f, 0.f, 0.f, 0.f};
      sfor<4>([&](auto qd) {
        vmcnt<(qd.value == 0 ? 8 : 12 - 4 * qd.value)>();
        BAR();
        const u32 a = L + WQ + tra + ((qd.value + j) & 3) * 32768;
        v4u wf[4];
#pragma unroll
        for (int ks = 0; ks < 4; ++ks) wf[ks] = trfrag(a + ks * 8192, a + ks * 8192 + 1024);
        if (qd.value == 3 && w < 7) {
#pragma unroll
          for (int s = 0; s < 4; ++s) kf[s] = lds128(L + WUK + (16 * w + i) * 256 + (((4 * s + g) ^ fsw(i)) * 16));
        }
        SCHED();
        d0 = mfma32(wf[0], xn[4 * qd.value], d0); d1 = mfma32(wf[1], xn[4 * qd.value + 1], d1);
        d0 = mfma32(wf[2], xn[4 * qd.value + 2], d0); d1 = mfma32(wf[3], xn[4 * qd.value + 3], d1);
        SCHED();
        if (qd.value == 0) WQ_DMA(3);
      });
      st64(L + WUK + w * 4096 + q * 32 + g * 8, pk4(d0 + d1));
    }
    BAR();
#define QF_ADDR(s) (L + WUK + (2 * (s) + (g >> 1)) * 4096 + q * 32 + (g & 1) * 16)
    {
      f4 d = {0.f, 0.f, 0.f, 0.f};
      v4u qf[4];
#pragma unroll
      for (int s = 0; s < 4; ++s) qf[s] = lds128(QF_ADDR(s));
      SCHED();
#pragma unroll
      for (int s = 0; s < 4; ++s) d = mfma32(kf[s], qf[s], d);
      constexpr float SC = 0.08838834764831845f * 1.4426950408889634f;
      d *= SC;
      st64(L + QAS + q * 256 + (16 * w + 4 * g) * 2,
           v2u{(__builtin_amdgcn_perm(asu(d[1]) + 0x10000u, asu(d[0]) + 0x10000u, 0x07060302) & 0xFFFEFFFEu) | tb0,
               (__builtin_amdgcn_perm(asu(d[3]) + 0x10000u, asu(d[2]) + 0x10000u, 0x07060302) & 0xFFFEFFFEu) | tb1});
    }
    BAR();
    {
      const u32 set = ((h >> 2) << 6) | (j << 4) | ((h & 1) << 3) | (((h >> 1) & 1) << 2) | p;
      if (w == 0) STW128(lds128(L + QAS + q * 256 + (4 * g + p) * 16), WS_QA + (q * 4 + g) * 8192 + set * 16);
    }
    BAR();
    UNTIL(T_WT);
    {
      const v4u s2 = mk_srd(wuv, 512u * 8192);
#pragma unroll
      for (int n = 0; n < 4; ++n) dma16<false>(s2, n < 2 ? vo0 : vo1, so + 4 * n * 8192, L + WUV + (16 * w + 4 * n) * 256);
      const v4u s = mk_srd(wo, 4096u * 1024);
      const u32 vo = (lane >> 3) * 1024 + (((lane & 7) ^ (g & 1)) * 16);
      const u32 wb = (w + 2 * j) & 7;
#pragma unroll
      for (int n = 0; n < 16; ++n) {
        dma16<false>(s, vo, (h * 128 + 8 * n) * 1024 + wb * 128, L + WO + wb * 16384 + n * 1024);
      }
    }
    VMCNT(16);
    BAR();
    v4u uv[4], of[4][4];
#pragma unroll
    for (int ks = 0; ks < 4; ++ks) uv[ks] = trfrag(L + WUV + tra + ks * 8192, L + WUV + tra + ks * 8192 + 1024);
    BAR();
    VMCNT(0);
    BAR();
#define OF_READ()                                                                                                              \
  _Pragma("unroll") for (int ct = 0; ct < 4; ++ct) {                                                                           \
    const u32 a = L + WO + w * 16384 + (8 * g + q) * 128 + (((2 * p + (ct >> 1)) ^ ((q >> 1) & 1)) * 16) + 8 * (ct & 1);       \
    _Pragma("unroll") for (int ks = 0; ks < 4; ++ks) of[ct][ks] = trfrag(a + ks * 4096, a + ks * 4096 + 512);                  \
  }
    const u32 bb = w >> 1, sh = w & 1;
    u32 t8 = 0;
    u32 oa[4], cw = L + CIMG + q * 272 + (16 * w + 4 * g) * 2, ca = L + CIMG + q * 272 + g * 16;
    u32 apo = WS_AP + (q * 32 + 4 * w + g) * 8192 + u * 64 + p * 16;
    u32 col = (u >> 5) * 2048 + ((u & 31) * 16 + 4 * p) * 4, gro = u * 8192 + w * 1024 + lane * 16;
#pragma unroll
    for (int ks = 0; ks < 4; ++ks) oa[ks] = L + OIMG + i * 256 + (((4 * ks + g) ^ fsw(i)) * 16);
    {
      Regs<4> po;
      u32 po_off = WS_PO + (u * 128 + w * 16) * 256 + lane * 16;
      const u32 row = 4 * bb + 2 * sh + (g & 1);
      u32 ow = L + OIMG + row * 256 + ((((i & 12) + 2 * (g >> 1) + (p >> 1)) ^ fsw(row)) * 16) + 8 * (p & 1), wa = L + WGT + (16 * w + g) * 4;
      PIN(po_off); PIN(ow); PIN(wa); PIN(cw); PIN(ca); PIN(apo); PIN(col); PIN(gro);
#pragma unroll
      for (int ks = 0; ks < 4; ++ks) PIN(oa[ks]);
      float wgt[4];
      u32 half = 0x8000u, hmask = 0xFFFF0000u;
      asm volatile("" : "+v"(half), "+s"(hmask));
      {
        Regs<2> s2;
        if (w == 0) {
          gather<2, 64 * 512>(rs, WS_ST + lane * 512 + h * 16, v4u{0u, tag32, 0u, tag32}, v4u{0u, ~0u, 0u, ~0u}, s2);
          SLEEP(S_PO);
        }
        asm volatile("s_barrier" ::: "memory");
        sfor<4>([&](auto n) { AT(po, n) = LDW2(po_off, n.value * 1024); });
        t8 = NOW();
        if (w == 0) {
          sfor<2>([&](auto n) {
            const float m = asf(AT(s2, n).x), M = max16x(rowmax16(m));
            const float e = __builtin_amdgcn_exp2f(m - M);
            st32f(L + WGT + n.value * 256 + lane * 4, e * __builtin_amdgcn_rcpf(sum16x(rowsum16(e * asf(AT(s2, n).z)))));
          });
        }
        SCHED();
        OF_READ();
        BAR();
#pragma unroll
        for (int n = 0; n < 4; ++n) wgt[n] = lds32f(wa + 16 * n);
      }
      SCHED();
      gather<4, 1024, true>(rs, po_off, v4u{tb0, tb1, tb0, tb1}, v4u{0x10001u, 0x10001u, 0x10001u, 0x10001u}, po);
      f2 acc[4];
      sfor<4>([&](auto n) {
        const v4u t = AT(po, n);
        const f2 wv = {wgt[n.value], wgt[n.value]};
#pragma unroll
        for (int k = 0; k < 4; ++k) {
          const f2 v = {asf((t[k] << 16) | half), asf((t[k] & hmask) | half)};
          acc[k] = n.value == 0 ? v * wv : __builtin_elementwise_fma(v, wv, acc[k]);
        }
      });
      f4 r4;
#pragma unroll
      for (int k = 0; k < 4; ++k) {
        auto r = __builtin_amdgcn_permlane32_swap(asu(acc[k >> 1][k & 1]), asu(acc[2 + (k >> 1)][k & 1]), false, false);
        r4[k] = asf(r[0]) + asf(r[1]);
      }
      st64(ow, pk4(r4));
    }
    BAR();
    {
      f4 d = {0.f, 0.f, 0.f, 0.f};
      v4u bf[4];
#pragma unroll
      for (int ks = 0; ks < 4; ++ks) bf[ks] = lds128(oa[ks]);
      SCHED();
#pragma unroll
      for (int ks = 0; ks < 4; ++ks) d = mfma32(uv[ks], bf[ks], d);
#pragma unroll
      for (int e = 0; e < 4; ++e) { d[e] += qp<0xB1>(d[e]); d[e] += qp<0x4E>(d[e]); }
      st64(cw, pk4(d));
    }
    BAR();
    {
      f4 d[4];
#pragma unroll
      for (int ct = 0; ct < 4; ++ct) d[ct] = f4{0.f, 0.f, 0.f, 0.f};
      v4u cf[4];
#pragma unroll
      for (int ks = 0; ks < 4; ++ks) cf[ks] = lds128(ca + ks * 64);
      SCHED();
#pragma unroll
      for (int ks = 0; ks < 4; ++ks)
#pragma unroll
        for (int ct = 0; ct < 4; ++ct) d[ct] = mfma32(of[ct][ks], cf[ks], d[ct]);
      const f4 r = sel4(p & 2, sel4(p & 1, d[3], d[2]), sel4(p & 1, d[1], d[0]));
      STW128(enc24v(r, tag8), apo);
    }
    constexpr u32 RS = 8704;
    u32 rsw = L + RS + w * 64 + p * 16, rsr = L + RS + p * 16, h1o = WS_H1 + (lane >> 2) * 8192 + col;
    PIN(rsw); PIN(rsr); PIN(h1o);
    Regs<1> a1;
    UNTIL(t8 + D_AP);
    if (w == 1 && slot == 0 && lane == 0) __builtin_amdgcn_raw_buffer_store_b64(v2u{(u32)__builtin_amdgcn_s_memrealtime(), tag32}, rs, WS_HINT + xcd * 256, 0, 16);
    gather<1, 0, false, WS_AP>(rs, gro, key8, msk8, a1);
    st128(rsw, __builtin_bit_cast(v4u, lane_reduce(__builtin_bit_cast(f4, a1.h))));
    BAR();
    f4 h1 = {0.f, 0.f, 0.f, 0.f};
    if (w == 0) {
      f4 t[8];
#pragma unroll
      for (int n = 0; n < 8; ++n) t[n] = lds128f(rsr + 64 * n);
      SCHED();
      h1 = ((t[0] + t[1]) + (t[2] + t[3])) + ((t[4] + t[5]) + (t[6] + t[7]));
      h1 += f4{bflo(xres.x), bfhi(xres.x), bflo(xres.y), bfhi(xres.y)};
      if (lane < 32) STW128(enc24v(h1, tag8), h1o);
    }
    SLEEP(S_H1);
    gather<1, 0>(rs, WS_H1 + xcd * 8192 + w * 1024 + lane * 16, key8, msk8, a1);
    BAR();
    SLEEP(S_FP);
    if (w < 7) {
      gather<1, 0, false, WS_FP>(rs, gro, key8, msk8, a1);
      st128(rsw + 512, __builtin_bit_cast(v4u, lane_reduce(__builtin_bit_cast(f4, a1.h))));
    }
    BAR();
    if (w == 0) {
      f4 t[7];
#pragma unroll
      for (int n = 0; n < 7; ++n) t[n] = lds128f(rsr + 512 + 64 * n);
      SCHED();
      const f4 y = h1 + (((t[0] + t[1]) + (t[2] + t[3])) + ((t[4] + t[5]) + t[6]));
      if (lane < 4) *(v2u*)(out + (col >> 2)) = pk4(y);
      if (u == 0 && lane == 0) __hip_atomic_store((u32*)ws, tag32, __ATOMIC_RELAXED, __HIP_MEMORY_SCOPE_AGENT);
    }
  }
}

extern "C" void kernel_launch(void* const* d_in, const int* in_sizes, int n_in, void* d_out, int out_size, void* d_ws, size_t ws_size,
                              hipStream_t stream) {
  (void)hipFuncSetAttribute((const void*)fused_decode, hipFuncAttributeMaxDynamicSharedMemorySize, (int)LDS_BYTES);
  hipLaunchKernelGGL(fused_decode, dim3(256), dim3(512), LDS_BYTES, stream, (const u16*)d_in[0], (const u16*)d_in[1], (const u16*)d_in[2],
                     (const u16*)d_in[3], (const u16*)d_in[4], (const u16*)d_in[5], (const u16*)d_in[7], (const u16*)d_in[8],
                     (const u16*)d_in[9], (u16*)d_out, (unsigned char*)d_ws);
}
```
